```python
import torch

def _softmax(x, axis=-1):
    return torch.softmax(x, dim=axis)
def _gelu(x):
    return torch.nn.functional.gelu(x, approximate="tanh")
def _silu(x):
    return torch.nn.functional.silu(x)
def _sigmoid(x):
    return torch.sigmoid(x)
def _layernorm(x, axis=-1, eps=1e-5):
    m = x.mean(dim=axis, keepdim=True)
    v = x.var(dim=axis, keepdim=True, unbiased=False)
    return (x - m) / torch.sqrt(v + eps)
def _scatter_add(tgt, idx, src, axis=0):
    return tgt.clone().index_add_(axis, idx, src)
def _segment_sum(src, seg, n):
    out = torch.zeros((n,) + src.shape[1:], dtype=src.dtype)
    return out.index_add_(0, seg, src)
def _pad(x, pw):
    flat = tuple(v for pair in reversed(pw) for v in pair)
    return torch.nn.functional.pad(x, flat)

kernel_name = 'standalone__gqa_block_cached__sampling'

def setup_inputs():

    _w = (lambda a: (lambda t: t.to(torch.bfloat16) if t.is_floating_point() else t)(torch.as_tensor(a)))
    B, Sq, D = 32, 16, 1024
    Hq, Hkv, Sk, Dh = 32, 4, 2048, 128
    assert Hq % Hkv == 0, (Hq, Hkv)
    return {
        "x": _w((lambda *s: torch.randn(*s, dtype=torch.bfloat16) * 0.8)(B, Sq, D)),
        "k_cache": _w((lambda *s: torch.randn(*s, dtype=torch.bfloat16) * 0.8)(B, Hkv, Sk, Dh)),
        "v_cache": _w((lambda *s: torch.randn(*s, dtype=torch.bfloat16) * 0.05)(B, Hkv, Sk, Dh)),
        "wq": _w((lambda *s: torch.randn(*s, dtype=torch.bfloat16) * 0.8)(D, Hq * Dh) * (D ** -0.5)),
        "wo": _w((lambda *s: torch.randn(*s, dtype=torch.bfloat16) * 0.05)(Hq * Dh, D) * ((Hq * Dh) ** -0.5)),
    }

def reference(x, k_cache, v_cache, wq, wo):
    B, Sq, D = x.shape
    Hq, Dh = 32, 128
    Hkv = k_cache.shape[1]
    rep = Hq // Hkv
    q = (lambda x, *d: x.permute(*d))((x @ wq).reshape(B, Sq, Hq, Dh), 0, 2, 1, 3)
    k = (lambda x, n, axis: torch.repeat_interleave(x, n, dim=axis))(k_cache, rep, axis=1)
    v = (lambda x, n, axis: torch.repeat_interleave(x, n, dim=axis))(v_cache, rep, axis=1)
    scores = torch.einsum("bhqd,bhkd->bhqk", q, k) * (Dh ** -0.5)
    p = _softmax(scores, axis=-1)
    o = torch.einsum("bhqk,bhkd->bhqd", p, v)
    return (lambda x, *d: x.permute(*d))(o, 0, 2, 1, 3).reshape(B, Sq, Hq * Dh) @ wo
```

```cpp
#include <hip/hip_runtime.h>
#include <hip/hip_bf16.h>
#define KV_NT 1
typedef unsigned v4u __attribute__((ext_vector_type(4)));
typedef unsigned v2u __attribute__((ext_vector_type(2)));
typedef float f32x16 __attribute__((ext_vector_type(16)));
typedef float f32x4 __attribute__((ext_vector_type(4)));
typedef float f32x2 __attribute__((ext_vector_type(2)));
typedef __bf16 bf16x8 __attribute__((ext_vector_type(8)));
typedef __bf16 bf16x4 __attribute__((ext_vector_type(4)));
typedef __bf16 bf16x2 __attribute__((ext_vector_type(2)));
#define DI __device__ __forceinline__
DI unsigned rfl(unsigned x) { return __builtin_amdgcn_readfirstlane(x); }
DI unsigned cvtpk(float lo, float hi) { f32x2 v = {lo, hi}; bf16x2 b = __builtin_convertvector(v, bf16x2); return __builtin_bit_cast(unsigned, b); }
template <int NT> DI void dma16(const void* g, unsigned ldsaddr) {
  if constexpr (NT) asm volatile("s_mov_b32 m0, %1\n\ts_nop 0\n\tglobal_load_lds_dwordx4 %0, off nt" :: "v"(g), "s"(ldsaddr) : "memory");
  else asm volatile("s_mov_b32 m0, %1\n\ts_nop 0\n\tglobal_load_lds_dwordx4 %0, off" :: "v"(g), "s"(ldsaddr) : "memory");
}
template <int N> DI void waitvm() { asm volatile("s_waitcnt vmcnt(%0)" :: "i"(N) : "memory"); }
DI bf16x4 trread(unsigned a) { bf16x4 r; asm volatile("ds_read_b64_tr_b16 %0, %1" : "=v"(r) : "v"(a) : "memory"); return r; }
DI bf16x8 ldsread128(unsigned a) { bf16x8 r; asm volatile("ds_read_b128 %0, %1" : "=v"(r) : "v"(a) : "memory"); return r; }
#define SB() __builtin_amdgcn_sched_barrier(0)

constexpr int B = 32, SQ = 16, D = 1024, HQ = 32, HKV = 4, SK = 2048, DH = 128;
DI unsigned offb(unsigned row, unsigned ch) { return 256u * row + 16u * (ch ^ (((row & 3) << 2) | ((row >> 2) & 3))); }

template <int MODE>
__global__ __launch_bounds__(256) void gemm64x128(const unsigned short* __restrict__ A, const unsigned short* __restrict__ Bm, void* __restrict__ C) {
  constexpr int lda = MODE == 0 ? 1024 : 4096, ldb = MODE == 0 ? 4096 : 1024, KL = 1024;
  __shared__ __attribute__((aligned(16))) char sA[2][64 * 128 * 2];
  __shared__ __attribute__((aligned(16))) char sB[2][128 * 256];
  const int tid = threadIdx.x, lane = tid & 63, wave = tid >> 6;
  int mt, nt, ks;
  if (MODE == 0) { mt = blockIdx.x & 7; nt = blockIdx.x >> 3; ks = 0; } else { mt = blockIdx.x & 7; nt = (blockIdx.x >> 3) & 7; ks = blockIdx.x >> 6; }
  const unsigned short* Ap = A + (size_t)mt * 64 * lda + ks * KL;
  const unsigned short* Bp = Bm + (size_t)ks * KL * ldb + nt * 128;
  v4u ra[4], rb[8];
  auto gload = [&](int kt) {
#pragma unroll
    for (int i = 0; i < 4; ++i) { int n = tid + 256 * i, row = n >> 4, ch = n & 15; ra[i] = *(const v4u*)(Ap + (size_t)row * lda + kt * 128 + ch * 8); }
#pragma unroll
    for (int i = 0; i < 8; ++i) { int n = tid + 256 * i, row = n >> 4, ch = n & 15; rb[i] = *(const v4u*)(Bp + (size_t)(kt * 128 + row) * ldb + ch * 8); }
  };
  auto swrite = [&](int buf) {
#pragma unroll
    for (int i = 0; i < 4; ++i) { int n = tid + 256 * i, row = n >> 4, ch = n & 15; *(v4u*)(sA[buf] + offb(row, ch)) = ra[i]; }
#pragma unroll
    for (int i = 0; i < 8; ++i) { int n = tid + 256 * i, row = n >> 4, ch = n & 15; *(v4u*)(sB[buf] + offb(row, ch)) = rb[i]; }
  };
  const int mb = wave & 1, nb = wave >> 1;
  f32x16 acc[2] = {};
  gload(0); swrite(0); __syncthreads();
  for (int kt = 0; kt < KL / 128; ++kt) {
    const int buf = kt & 1;
    if (kt + 1 < KL / 128) gload(kt + 1);
    const unsigned aB = (unsigned)(size_t)sA[buf], bB = (unsigned)(size_t)sB[buf];
#pragma unroll
    for (int s = 0; s < 8; ++s) {
      bf16x8 af = ldsread128(aB + offb(32 * mb + (lane & 31), 2 * s + (lane >> 5)));
      bf16x4 t[2][2];
#pragma unroll
      for (int c = 0; c < 2; ++c)
#pragma unroll
        for (int tt = 0; tt < 2; ++tt) {
          const unsigned h = lane >> 5, blk = (lane >> 4) & 1, q = (lane & 15) >> 2, p = lane & 3;
          t[c][tt] = trread(bB + offb(16 * s + 8 * h + 4 * tt + q, 4 * (2 * nb + c) + 2 * blk + (p >> 1)) + 8 * (p & 1));
        }
      asm volatile("s_waitcnt lgkmcnt(0)" : "+v"(af), "+v"(t[0][0]), "+v"(t[0][1]), "+v"(t[1][0]), "+v"(t[1][1]) :: "memory");
#pragma unroll
      for (int c = 0; c < 2; ++c) { bf16x8 wf = __builtin_shufflevector(t[c][0], t[c][1], 0, 1, 2, 3, 4, 5, 6, 7);
        acc[c] = __builtin_amdgcn_mfma_f32_32x32x16_bf16(wf, af, acc[c], 0, 0, 0); }
    }
    if (kt + 1 < KL / 128) swrite(buf ^ 1);
    __syncthreads();
  }
  const int m = mt * 64 + 32 * mb + (lane & 31), hi = lane >> 5;
#pragma unroll
  for (int c = 0; c < 2; ++c)
#pragma unroll
    for (int g = 0; g < 4; ++g) {
      const int n = nt * 128 + 32 * (2 * nb + c) + 8 * g + 4 * hi;
      if (MODE == 0) { v2u w = {cvtpk(acc[c][4 * g], acc[c][4 * g + 1]), cvtpk(acc[c][4 * g + 2], acc[c][4 * g + 3])}; *(v2u*)((unsigned short*)C + (size_t)m * 4096 + n) = w; }
      else { f32x4 w = {acc[c][4 * g], acc[c][4 * g + 1], acc[c][4 * g + 2], acc[c][4 * g + 3]}; *(f32x4*)((float*)C + ((size_t)ks * 512 + m) * 1024 + n) = w; }
    }
}
__global__ __launch_bounds__(256) void reduce4(const float* __restrict__ P, unsigned short* __restrict__ out) {
  const int i = (blockIdx.x * 256 + threadIdx.x) * 4;
  f32x4 a = *(const f32x4*)(P + i), b = *(const f32x4*)(P + 524288 + i), c = *(const f32x4*)(P + 2 * 524288 + i), d = *(const f32x4*)(P + 3 * 524288 + i);
  a = (a + b) + (c + d);
  v2u w = {cvtpk(a[0], a[1]), cvtpk(a[2], a[3])}; *(v2u*)(out + i) = w;
}

constexpr int NSLOT = 4;
__global__ __launch_bounds__(256, 1) void attn_hs(const unsigned short* __restrict__ Q, const unsigned short* __restrict__ K, const unsigned short* __restrict__ V, unsigned short* __restrict__ O) {
  extern __shared__ __attribute__((aligned(16))) char lds[];
  const int tid = threadIdx.x, lane = tid & 63, wave = rfl(tid >> 6), l32 = lane & 31, hi = lane >> 5;
  const int w = blockIdx.x, g = w & 7, r = w >> 3, pair = g * 16 + (r >> 1), hh = r & 1, b = pair >> 2, kvh = pair & 3;
  const char* Kp = (const char*)K + (size_t)pair * (SK * DH * 2) + (size_t)wave * (512 * 256);
  const char* Vp = (const char*)V + (size_t)pair * (SK * DH * 2) + (size_t)wave * (512 * 256);
  const unsigned lbase = (unsigned)(size_t)lds + wave * (NSLOT * 8192);
  unsigned srcoff[4];
#pragma unroll
  for (int i = 0; i < 4; ++i) { const unsigned row = 4 * i + (lane >> 4); srcoff[i] = 256u * (lane >> 4) + 16u * ((lane & 15) ^ (((row & 3) << 2) | ((row >> 2) & 3))); }
  auto issue = [&](int p) {
    const char* src = ((p & 1) ? Vp : Kp) + (size_t)(p >> 1) * 8192;
    const unsigned dst = lbase + (p % NSLOT) * 8192;
#pragma unroll
    for (int i = 0; i < 8; ++i) dma16<KV_NT>(src + i * 1024 + srcoff[i & 3], rfl(dst + i * 1024));
  };
  for (int p = 0; p < NSLOT; ++p) issue(p);
  bf16x8 qf[2][8];
#pragma unroll
  for (int qb = 0; qb < 2; ++qb) { const int rho = qb * 32 + l32, head = kvh * 8 + hh * 4 + (rho >> 4), pos = rho & 15;
    const unsigned short* qp = Q + ((size_t)(b * 16 + pos) * 4096 + head * 128 + 8 * hi);
#pragma unroll
    for (int s = 0; s < 8; ++s) qf[qb][s] = *(const bf16x8*)(qp + 16 * s); }
  asm volatile("s_waitcnt vmcnt(0)" ::: "memory");
  f32x16 o[2][4] = {};
  float m[2] = {-1e30f, -1e30f}, l[2] = {0.f, 0.f};
  const float c = 0.088388347648318447f * 1.4426950408889634f;
  unsigned kaddr[8], vaddr[4][2];
#pragma unroll
  for (int s = 0; s < 8; ++s) kaddr[s] = offb(l32, 2 * s + hi);
  { const unsigned blk = (lane >> 4) & 1, q = (lane & 15) >> 2, p = lane & 3;
#pragma unroll
    for (int db = 0; db < 4; ++db)
#pragma unroll
      for (int t = 0; t < 2; ++t) vaddr[db][t] = offb(8 * t + 4 * hi + q, 4 * db + 2 * blk + (p >> 1)) + 8 * (p & 1); }
  for (int t = 0; t < 16; ++t) {
    if (t < 15) waitvm<8 * (NSLOT - 1)>(); else waitvm<8>();
    const unsigned kb = lbase + ((2 * t) % NSLOT) * 8192, vb = lbase + ((2 * t + 1) % NSLOT) * 8192;
    bf16x8 kf[8];
#pragma unroll
    for (int s = 0; s < 8; ++s) kf[s] = ldsread128(kb + kaddr[s]);
    asm volatile("s_waitcnt lgkmcnt(0)" : "+v"(kf[0]), "+v"(kf[1]), "+v"(kf[2]), "+v"(kf[3]), "+v"(kf[4]), "+v"(kf[5]), "+v"(kf[6]), "+v"(kf[7]) :: "memory");
    if (2 * t + NSLOT < 32) issue(2 * t + NSLOT);
    f32x16 sc[2] = {};
#pragma unroll
    for (int s = 0; s < 8; ++s) { sc[0] = __builtin_amdgcn_mfma_f32_32x32x16_bf16(kf[s], qf[0][s], sc[0], 0, 0, 0); sc[1] = __builtin_amdgcn_mfma_f32_32x32x16_bf16(kf[s], qf[1][s], sc[1], 0, 0, 0); }
    if (t < 14) waitvm<8 * (NSLOT - 1)>(); else if (t == 14) waitvm<16>(); else waitvm<0>();
    bf16x4 vt[4][2][2];
#pragma unroll
    for (int db = 0; db < 4; ++db)
#pragma unroll
      for (int s = 0; s < 2; ++s)
#pragma unroll
        for (int tt = 0; tt < 2; ++tt) vt[db][s][tt] = trread(vb + vaddr[db][tt] + 4096 * s);
    bf16x8 pf[2][2];
#pragma unroll
    for (int qb = 0; qb < 2; ++qb) {
      float mx = sc[qb][0];
#pragma unroll
      for (int i = 1; i < 16; ++i) mx = fmaxf(mx, sc[qb][i]);
      { auto rr = __builtin_amdgcn_permlane32_swap(__float_as_uint(mx), __float_as_uint(mx), false, false); mx = fmaxf(__uint_as_float(rr[0]), __uint_as_float(rr[1])); }
      mx *= c;
      const float mn = fmaxf(m[qb], mx);
      if (__any(mn > m[qb])) { const float al = __builtin_amdgcn_exp2f(m[qb] - mn); l[qb] *= al;
#pragma unroll
        for (int db = 0; db < 4; ++db) o[qb][db] *= al;
        m[qb] = mn; }
      float ps = 0.f;
#pragma unroll
      for (int i = 0; i < 16; ++i) { sc[qb][i] = __builtin_amdgcn_exp2f(fmaf(sc[qb][i], c, -mn)); ps += sc[qb][i]; }
      l[qb] += ps;
#pragma unroll
      for (int s = 0; s < 2; ++s) { v4u wv = {cvtpk(sc[qb][8 * s], sc[qb][8 * s + 1]), cvtpk(sc[qb][8 * s + 2], sc[qb][8 * s + 3]), cvtpk(sc[qb][8 * s + 4], sc[qb][8 * s + 5]), cvtpk(sc[qb][8 * s + 6], sc[qb][8 * s + 7])};
        pf[qb][s] = __builtin_bit_cast(bf16x8, wv); }
    }
    asm volatile("s_waitcnt lgkmcnt(0)" : "+v"(vt[0][0][0]), "+v"(vt[0][0][1]), "+v"(vt[0][1][0]), "+v"(vt[0][1][1]), "+v"(vt[1][0][0]), "+v"(vt[1][0][1]), "+v"(vt[1][1][0]), "+v"(vt[1][1][1]),
                 "+v"(vt[2][0][0]), "+v"(vt[2][0][1]), "+v"(vt[2][1][0]), "+v"(vt[2][1][1]), "+v"(vt[3][0][0]), "+v"(vt[3][0][1]), "+v"(vt[3][1][0]), "+v"(vt[3][1][1]) :: "memory");
    if (2 * t + 1 + NSLOT < 32) issue(2 * t + 1 + NSLOT);
#pragma unroll
    for (int s = 0; s < 2; ++s)
#pragma unroll
      for (int db = 0; db < 4; ++db) { bf16x8 vf = __builtin_shufflevector(vt[db][s][0], vt[db][s][1], 0, 1, 2, 3, 4, 5, 6, 7);
        o[0][db] = __builtin_amdgcn_mfma_f32_32x32x16_bf16(vf, pf[0][s], o[0][db], 0, 0, 0);
        o[1][db] = __builtin_amdgcn_mfma_f32_32x32x16_bf16(vf, pf[1][s], o[1][db], 0, 0, 0); }
  }
#pragma unroll
  for (int qb = 0; qb < 2; ++qb) { auto rr = __builtin_amdgcn_permlane32_swap(__float_as_uint(l[qb]), __float_as_uint(l[qb]), false, false); l[qb] = __uint_as_float(rr[0]) + __uint_as_float(rr[1]); }
  __syncthreads();
  float* ml = (float*)(lds + 98304);
  if (hi == 0) { for (int qb = 0; qb < 2; ++qb) { ml[((wave * 2 + qb) * 32 + l32) * 2] = m[qb]; ml[((wave * 2 + qb) * 32 + l32) * 2 + 1] = l[qb]; } }
  __syncthreads();
#pragma unroll
  for (int qb = 0; qb < 2; ++qb) {
    float mm = -1e30f; for (int x = 0; x < 4; ++x) mm = fmaxf(mm, ml[((x * 2 + qb) * 32 + l32) * 2]);
    float ll = 0.f; for (int x = 0; x < 4; ++x) ll += ml[((x * 2 + qb) * 32 + l32) * 2 + 1] * __builtin_amdgcn_exp2f(ml[((x * 2 + qb) * 32 + l32) * 2] - mm);
    const float f = __builtin_amdgcn_exp2f(m[qb] - mm) / ll;
#pragma unroll
    for (int db = 0; db < 4; ++db) o[qb][db] *= f;
  }
  f32x4* ex = (f32x4*)lds;
#pragma unroll
  for (int db = 0; db < 4; ++db) if (db != wave) { const int si = wave - (wave > db);
#pragma unroll
    for (int qb = 0; qb < 2; ++qb)
#pragma unroll
      for (int g4 = 0; g4 < 4; ++g4) ex[(((db * 3 + si) * 2 + qb) * 4 + g4) * 64 + lane] = (f32x4){o[qb][db][4 * g4], o[qb][db][4 * g4 + 1], o[qb][db][4 * g4 + 2], o[qb][db][4 * g4 + 3]}; }
  __syncthreads();
#pragma unroll
  for (int qb = 0; qb < 2; ++qb) {
    const int rho = qb * 32 + l32, head = kvh * 8 + hh * 4 + (rho >> 4), pos = rho & 15;
#pragma unroll
    for (int g4 = 0; g4 < 4; ++g4) {
      f32x4 a;
      if (wave == 0) a = (f32x4){o[qb][0][4 * g4], o[qb][0][4 * g4 + 1], o[qb][0][4 * g4 + 2], o[qb][0][4 * g4 + 3]};
      else if (wave == 1) a = (f32x4){o[qb][1][4 * g4], o[qb][1][4 * g4 + 1], o[qb][1][4 * g4 + 2], o[qb][1][4 * g4 + 3]};
      else if (wave == 2) a = (f32x4){o[qb][2][4 * g4], o[qb][2][4 * g4 + 1], o[qb][2][4 * g4 + 2], o[qb][2][4 * g4 + 3]};
      else a = (f32x4){o[qb][3][4 * g4], o[qb][3][4 * g4 + 1], o[qb][3][4 * g4 + 2], o[qb][3][4 * g4 + 3]};
#pragma unroll
      for (int si = 0; si < 3; ++si) a += ex[(((wave * 3 + si) * 2 + qb) * 4 + g4) * 64 + lane];
      v2u wv = {cvtpk(a[0], a[1]), cvtpk(a[2], a[3])};
      *(v2u*)(O + (size_t)(b * 16 + pos) * 4096 + head * 128 + 32 * wave + 8 * g4 + 4 * hi) = wv;
    }
  }
}

extern "C" void kernel_launch(void* const* d_in, const int* in_sizes, int n_in, void* d_out, int out_size, void* d_ws, size_t ws_size, hipStream_t stream) {
  const unsigned short* x = (const unsigned short*)d_in[0]; const unsigned short* k = (const unsigned short*)d_in[1]; const unsigned short* v = (const unsigned short*)d_in[2];
  const unsigned short* wq = (const unsigned short*)d_in[3]; const unsigned short* wo = (const unsigned short*)d_in[4];
  unsigned short* q = (unsigned short*)d_ws; unsigned short* o = q + 512 * 4096; float* part = (float*)(o + 512 * 4096);
  hipFuncSetAttribute((const void*)attn_hs, hipFuncAttributeMaxDynamicSharedMemorySize, NSLOT * 8192 * 4);
  gemm64x128<0><<<256, 256, 0, stream>>>(x, wq, q);
  attn_hs<<<256, 256, NSLOT * 8192 * 4, stream>>>(q, k, v, o);
  gemm64x128<1><<<256, 256, 0, stream>>>(o, wo, part);
  reduce4<<<512, 256, 0, stream>>>(part, (unsigned short*)d_out);
}
```

```cpp
#include <hip/hip_runtime.h>
#include <hip/hip_bf16.h>

__global__ __launch_bounds__(256, 1) void gqa_block_fused(const void* x, const void* k, const void* v, const void* wq, const void* wo, void* out, void* ws) {
  __shared__ __attribute__((aligned(16))) char lds[163840];
  asm volatile(
    "s_load_dwordx8 s[4:11], %[ka], 0x0\n"
    "s_load_dwordx4 s[12:15], %[ka], 0x20\n"
    "s_load_dwordx2 s[16:17], %[ka], 0x30\n"
    "s_mov_b32 s24, %[bid]\n"
    "v_lshrrev_b32 v1, 6, %[tid]\n"
    "v_and_b32 v148, 63, %[tid]\n"
    "v_readfirstlane_b32 s25, v1\n"
    "s_and_b32 s26, s24, 7\n"
    "s_lshr_b32 s27, s24, 3\n"
    "s_lshr_b32 s83, s26, 1\n"
    "s_and_b32 s18, s27, 1\n"
    "s_lshl_b32 s96, s26, 4\n"
    "s_lshr_b32 s29, s27, 1\n"
    "s_add_u32 s96, s96, s29\n"
    "s_and_b32 s93, s27, 3\n"
    "s_mov_b32 s95, s18\n"
    "s_lshr_b32 s29, s27, 4\n"
    "s_lshl_b32 s29, s29, 1\n"
    "s_add_u32 s95, s95, s29\n"
    "s_and_b32 s94, s26, 1\n"
    "s_lshl_b32 s94, s94, 3\n"
    "s_and_b32 s29, s96, 7\n"
    "s_add_u32 s94, s94, s29\n"
    "s_and_b32 s19, s25, 1\n"
    "s_lshr_b32 s82, s25, 1\n"
    "v_lshrrev_b32 v150, 4, v148\n"
    "v_and_b32 v149, 15, v148\n"
    "v_lshlrev_b32 v2, 2, v150\n"
    "v_xor_b32 v2, v2, v149\n"
    "s_lshl_b32 s28, s25, 17\n"
    "v_lshl_add_u32 v3, v150, 13, s28\n"
    "v_xor_b32 v4, 0, v2\n"
    "v_lshl_add_u32 v4, v4, 4, v3\n"
    "v_add_u32 v161, 0, v4\n"
    "v_xor_b32 v4, 1, v2\n"
    "v_lshl_add_u32 v4, v4, 4, v3\n"
    "v_add_u32 v162, 31744, v4\n"
    "v_xor_b32 v4, 2, v2\n"
    "v_lshl_add_u32 v4, v4, 4, v3\n"
    "v_add_u32 v163, 63488, v4\n"
    "v_xor_b32 v4, 3, v2\n"
    "v_lshl_add_u32 v4, v4, 4, v3\n"
    "v_add_u32 v164, 95232, v4\n"
    "v_lshrrev_b32 v5, 3, v148\n"
    "v_and_b32 v6, 7, v148\n"
    "v_lshrrev_b32 v7, 1, v5\n"
    "v_xor_b32 v9, v7, v6\n"
    "s_lshl_b32 s28, s25, 15\n"
    "v_lshl_add_u32 v8, v5, 11, s28\n"
    "v_lshl_add_u32 v108, v9, 4, v8\n"
    "v_xor_b32 v9, 4, v9\n"
    "v_lshl_add_u32 v9, v9, 4, v8\n"
    "v_add_u32 v109, 15360, v9\n"
    "v_xor_b32 v1, v150, v149\n"
    "v_lshlrev_b32 v3, 8, v150\n"
    "v_xor_b32 v4, 0, v1\n"
    "v_lshl_add_u32 v124, v4, 4, v3\n"
    "v_xor_b32 v4, 4, v1\n"
    "v_lshl_add_u32 v125, v4, 4, v3\n"
    "v_xor_b32 v4, 8, v1\n"
    "v_lshl_add_u32 v126, v4, 4, v3\n"
    "v_xor_b32 v4, 12, v1\n"
    "v_lshl_add_u32 v127, v4, 4, v3\n"
    "v_lshlrev_b32 v1, 1, v150\n"
    "v_xor_b32 v1, v1, v149\n"
    "v_lshl_add_u32 v128, v1, 4, v3\n"
    "v_xor_b32 v129, 0x80, v128\n"
    "s_lshl_b32 s81, s25, 12\n"
    "s_lshl_b32 s80, s25, 11\n"
    "s_add_u32 s80, s80, 131072\n"
    "s_lshl_b32 s71, s82, 16\n"
    "s_lshl_b32 s29, s19, 12\n"
    "s_add_u32 s71, s71, s29\n"
    "s_lshr_b32 s28, s27, 2\n"
    "s_add_u32 s28, s28, s93\n"
    "s_and_b32 s28, s28, 3\n"
    "s_lshl_b32 s72, s28, 9\n"
    "s_lshl_b32 s73, s28, 21\n"
    "s_mov_b32 s66, 0\n"
    "s_mov_b32 s67, 0\n"
    "s_lshl_b32 s56, s26, 10\n"
    "s_lshl_b32 s29, s93, 8\n"
    "s_add_u32 s56, s56, s29\n"
    "s_sub_u32 s28, 0x800000, s56\n"
    "s_mov_b32 s38, s28\n"
    "s_mov_b32 s39, 0x00020000\n"
    "s_lshr_b32 s57, s27, 2\n"
    "s_lshl_b32 s57, s57, 17\n"
    "s_mov_b32 s22, 131072\n"
    "s_mov_b32 s23, 0x00020000\n"
    "s_mov_b32 s50, 17825792\n"
    "s_mov_b32 s51, 0x00020000\n"
    "s_lshl_b32 s28, s82, 13\n"
    "s_lshl_b32 s29, s19, 12\n"
    "s_add_u32 s28, s28, s29\n"
    "s_mul_i32 s30, s18, 131072\n"
    "s_add_u32 s30, s30, 196608\n"
    "s_sub_u32 s30, s30, s28\n"
    "s_mov_b32 s42, s30\n"
    "s_mov_b32 s43, 0x00020000\n"
    "s_mov_b32 s46, s30\n"
    "s_mov_b32 s47, 0x00020000\n"
    "s_mul_i32 s31, s18, 196608\n"
    "s_lshl_b32 s58, s96, 19\n"
    "s_add_u32 s58, s58, s31\n"
    "s_add_u32 s58, s58, s28\n"
    "v_mov_b32 v1, 0\n"
    "s_waitcnt lgkmcnt(0)\n"
    "s_add_u32 s36, s10, s56\n"
    "s_addc_u32 s37, s11, 0\n"
    "s_add_u32 s20, s4, s57\n"
    "s_addc_u32 s21, s5, 0\n"
    "s_add_u32 s40, s6, s58\n"
    "s_addc_u32 s41, s7, 0\n"
    "s_add_u32 s44, s8, s58\n"
    "s_addc_u32 s45, s9, 0\n"
    "s_mov_b64 s[48:49], s[16:17]\n"
    "s_cmp_lg_u32 s18, 1\n"
    "s_cbranch_scc1 .Lno_early\n"
    "s_add_u32 m0, s71, 0\n"
    "s_nop 0\n"
    "buffer_load_dwordx4 v124, s[40:43], s66 offen offset:0 nt lds\n"
    "buffer_load_dwordx4 v125, s[40:43], s66 offen offset:1024 nt lds\n"
    "buffer_load_dwordx4 v126, s[40:43], s66 offen offset:2048 nt lds\n"
    "buffer_load_dwordx4 v127, s[40:43], s66 offen offset:3072 nt lds\n"
    "s_add_u32 s66, s66, 16384\n"
    "s_add_u32 m0, s71, 8192\n"
    "s_nop 0\n"
    "buffer_load_dwordx4 v128, s[44:47], s67 offen offset:0 nt lds\n"
    "buffer_load_dwordx4 v129, s[44:47], s67 offen offset:1024 nt lds\n"
    "buffer_load_dwordx4 v128, s[44:47], s67 offen offset:2048 nt lds\n"
    "buffer_load_dwordx4 v129, s[44:47], s67 offen offset:3072 nt lds\n"
    "s_add_u32 s67, s67, 16384\n"
    "s_add_u32 m0, s71, 16384\n"
    "s_nop 0\n"
    "buffer_load_dwordx4 v124, s[40:43], s66 offen offset:0 nt lds\n"
    "buffer_load_dwordx4 v125, s[40:43], s66 offen offset:1024 nt lds\n"
    "buffer_load_dwordx4 v126, s[40:43], s66 offen offset:2048 nt lds\n"
    "buffer_load_dwordx4 v127, s[40:43], s66 offen offset:3072 nt lds\n"
    "s_add_u32 s66, s66, 16384\n"
    "s_add_u32 m0, s71, 24576\n"
    "s_nop 0\n"
    "buffer_load_dwordx4 v128, s[44:47], s67 offen offset:0 nt lds\n"
    "buffer_load_dwordx4 v129, s[44:47], s67 offen offset:1024 nt lds\n"
    "buffer_load_dwordx4 v128, s[44:47], s67 offen offset:2048 nt lds\n"
    "buffer_load_dwordx4 v129, s[44:47], s67 offen offset:3072 nt lds\n"
    "s_add_u32 s67, s67, 16384\n"
    ".Lno_early:\n"
    "s_add_u32 m0, s81, 32768\n"
    "s_nop 0\n"
    "buffer_load_dwordx4 v161, s[36:39], s73 offen offset:0 lds\n"
    "buffer_load_dwordx4 v162, s[36:39], s73 offen offset:1024 lds\n"
    "buffer_load_dwordx4 v163, s[36:39], s73 offen offset:2048 lds\n"
    "buffer_load_dwordx4 v164, s[36:39], s73 offen offset:3072 lds\n"
    "s_add_u32 m0, s80, 0\n"
    "s_add_u32 s73, s73, 0x80000\n"
    "buffer_load_dwordx4 v108, s[20:23], s72 offen offset:0 lds\n"
    "buffer_load_dwordx4 v109, s[20:23], s72 offen offset:1024 lds\n"
    "s_add_u32 s72, s72, 128\n"
    "s_and_b32 s73, s73, 0x7fffff\n"
    "s_and_b32 s72, s72, 0x7ff\n"
    "s_add_u32 m0, s81, 49152\n"
    "s_nop 0\n"
    "buffer_load_dwordx4 v161, s[36:39], s73 offen offset:0 lds\n"
    "buffer_load_dwordx4 v162, s[36:39], s73 offen offset:1024 lds\n"
    "buffer_load_dwordx4 v163, s[36:39], s73 offen offset:2048 lds\n"
    "buffer_load_dwordx4 v164, s[36:39], s73 offen offset:3072 lds\n"
    "s_add_u32 m0, s80, 8192\n"
    "s_add_u32 s73, s73, 0x80000\n"
    "buffer_load_dwordx4 v108, s[20:23], s72 offen offset:0 lds\n"
    "buffer_load_dwordx4 v109, s[20:23], s72 offen offset:1024 lds\n"
    "s_add_u32 s72, s72, 128\n"
    "s_and_b32 s73, s73, 0x7fffff\n"
    "s_and_b32 s72, s72, 0x7ff\n"
    "s_add_u32 m0, s81, 98304\n"
    "s_nop 0\n"
    "buffer_load_dwordx4 v161, s[36:39], s73 offen offset:0 lds\n"
    "buffer_load_dwordx4 v162, s[36:39], s73 offen offset:1024 lds\n"
    "buffer_load_dwordx4 v163, s[36:39], s73 offen offset:2048 lds\n"
    "buffer_load_dwordx4 v164, s[36:39], s73 offen offset:3072 lds\n"
    "s_add_u32 m0, s80, 16384\n"
    "s_add_u32 s73, s73, 0x80000\n"
    "buffer_load_dwordx4 v108, s[20:23], s72 offen offset:0 lds\n"
    "buffer_load_dwordx4 v109, s[20:23], s72 offen offset:1024 lds\n"
    "s_add_u32 s72, s72, 128\n"
    "s_and_b32 s73, s73, 0x7fffff\n"
    "s_and_b32 s72, s72, 0x7ff\n"
    "s_add_u32 m0, s81, 114688\n"
    "s_nop 0\n"
    "buffer_load_dwordx4 v161, s[36:39], s73 offen offset:0 lds\n"
    "buffer_load_dwordx4 v162, s[36:39], s73 offen offset:1024 lds\n"
    "buffer_load_dwordx4 v163, s[36:39], s73 offen offset:2048 lds\n"
    "buffer_load_dwordx4 v164, s[36:39], s73 offen offset:3072 lds\n"
    "s_add_u32 m0, s80, 24576\n"
    "s_add_u32 s73, s73, 0x80000\n"
    "buffer_load_dwordx4 v108, s[20:23], s72 offen offset:0 lds\n"
    "buffer_load_dwordx4 v109, s[20:23], s72 offen offset:1024 lds\n"
    "s_add_u32 s72, s72, 128\n"
    "s_and_b32 s73, s73, 0x7fffff\n"
    "s_and_b32 s72, s72, 0x7ff\n"
    "buffer_load_dword v154, v1, s[48:51], 0 offen sc1\n"
    "v_and_b32 v7, 3, v7\n"
    "v_lshlrev_b32 v7, 1, v7\n"
    "v_xor_b32 v7, v7, v6\n"
    "v_lshlrev_b32 v7, 4, v7\n"
    "v_lshl_add_u32 v144, v5, 11, v7\n"
    "v_add_u32 v145, 15360, v144\n"
    "v_add_u32 v146, 30720, v144\n"
    "v_add_u32 v147, 46080, v144\n"
    "s_lshl_b32 s68, s82, 20\n"
    "s_lshl_b32 s29, s19, 16\n"
    "s_add_u32 s68, s68, s29\n"
    "s_add_u32 s69, s68, 0x80000\n"
    "v_and_b32 v157, 31, v148\n"
    "v_lshrrev_b32 v158, 5, v148\n"
    "v_and_b32 v159, 1, v150\n"
    "v_bfe_u32 v151, v148, 2, 2\n"
    "v_and_b32 v152, 3, v148\n"
    "v_lshlrev_b32 v153, 4, v148\n"
    "s_lshl_b32 s28, s19, 12\n"
    "s_add_u32 s28, s28, 131072\n"
    "v_lshl_add_u32 v1, v157, 7, s28\n"
    "v_bfe_u32 v2, v157, 1, 3\n"
    "v_xor_b32 v2, v2, v158\n"
    "v_xor_b32 v3, 0, v2\n"
    "v_lshl_add_u32 v96, v3, 4, v1\n"
    "v_xor_b32 v3, 2, v2\n"
    "v_lshl_add_u32 v97, v3, 4, v1\n"
    "v_xor_b32 v3, 4, v2\n"
    "v_lshl_add_u32 v98, v3, 4, v1\n"
    "v_xor_b32 v3, 6, v2\n"
    "v_lshl_add_u32 v99, v3, 4, v1\n"
    "v_and_b32 v9, 1, v152\n"
    "v_lshrrev_b32 v10, 1, v152\n"
    "v_lshl_add_u32 v11, v159, 1, v9\n"
    "s_lshl_b32 s28, s82, 3\n"
    "v_lshlrev_b32 v1, 2, v151\n"
    "v_lshl_or_b32 v1, v158, 1, v1\n"
    "v_xor_b32 v1, v1, v11\n"
    "v_xor_b32 v1, s28, v1\n"
    "v_lshl_add_u32 v2, v158, 3, v151\n"
    "v_lshlrev_b32 v2, 8, v2\n"
    "v_lshl_add_u32 v2, v10, 3, v2\n"
    "v_xor_b32 v3, 0, v1\n"
    "v_lshl_add_u32 v3, v3, 4, v2\n"
    "v_add_u32 v100, 32768, v3\n"
    "v_add_u32 v104, 98304, v3\n"
    "v_xor_b32 v3, 1, v1\n"
    "v_lshl_add_u32 v3, v3, 4, v2\n"
    "v_add_u32 v101, 33792, v3\n"
    "v_add_u32 v105, 99328, v3\n"
    "v_xor_b32 v3, 4, v1\n"
    "v_lshl_add_u32 v3, v3, 4, v2\n"
    "v_add_u32 v102, 32768, v3\n"
    "v_add_u32 v106, 98304, v3\n"
    "v_xor_b32 v3, 5, v1\n"
    "v_lshl_add_u32 v3, v3, 4, v2\n"
    "v_add_u32 v103, 33792, v3\n"
    "v_add_u32 v107, 99328, v3\n"
    "v_mov_b64 v[16:17], 0\n"
    "v_mov_b64 v[18:19], 0\n"
    "v_mov_b64 v[20:21], 0\n"
    "v_mov_b64 v[22:23], 0\n"
    "v_mov_b64 v[24:25], 0\n"
    "v_mov_b64 v[26:27], 0\n"
    "v_mov_b64 v[28:29], 0\n"
    "v_mov_b64 v[30:31], 0\n"
    "v_mov_b64 v[32:33], 0\n"
    "v_mov_b64 v[34:35], 0\n"
    "v_mov_b64 v[36:37], 0\n"
    "v_mov_b64 v[38:39], 0\n"
    "v_mov_b64 v[40:41], 0\n"
    "v_mov_b64 v[42:43], 0\n"
    "v_mov_b64 v[44:45], 0\n"
    "v_mov_b64 v[46:47], 0\n"
    "s_lshl_b32 s30, s82, 16\n"
    "v_xor_b32 v1, v150, v149\n"
    "v_lshl_add_u32 v2, v149, 8, s30\n"
    "v_xor_b32 v3, 0, v1\n"
    "v_lshl_add_u32 v112, v3, 4, v2\n"
    "v_xor_b32 v3, 4, v1\n"
    "v_lshl_add_u32 v113, v3, 4, v2\n"
    "v_xor_b32 v3, 8, v1\n"
    "v_lshl_add_u32 v114, v3, 4, v2\n"
    "v_xor_b32 v3, 12, v1\n"
    "v_lshl_add_u32 v115, v3, 4, v2\n"
    "v_lshl_add_u32 v1, v159, 2, v151\n"
    "v_lshl_add_u32 v1, v1, 1, v10\n"
    "v_xor_b32 v1, s28, v1\n"
    "v_lshl_add_u32 v2, v150, 2, v151\n"
    "v_lshlrev_b32 v2, 8, v2\n"
    "v_lshl_add_u32 v2, v9, 3, v2\n"
    "s_add_u32 s30, s30, 8192\n"
    "v_add_u32 v2, s30, v2\n"
    "v_xor_b32 v3, 0, v1\n"
    "v_lshl_add_u32 v116, v3, 4, v2\n"
    "v_xor_b32 v3, 2, v1\n"
    "v_lshl_add_u32 v117, v3, 4, v2\n"
    "v_xor_b32 v3, 4, v1\n"
    "v_lshl_add_u32 v118, v3, 4, v2\n"
    "v_xor_b32 v3, 6, v1\n"
    "v_lshl_add_u32 v119, v3, 4, v2\n"
    "v_xor_b32 v3, 8, v1\n"
    "v_lshl_add_u32 v120, v3, 4, v2\n"
    "v_xor_b32 v3, 10, v1\n"
    "v_lshl_add_u32 v121, v3, 4, v2\n"
    "v_xor_b32 v3, 12, v1\n"
    "v_lshl_add_u32 v122, v3, 4, v2\n"
    "v_xor_b32 v3, 14, v1\n"
    "v_lshl_add_u32 v123, v3, 4, v2\n"
    "s_lshl_b32 s28, s94, 7\n"
    "s_sub_u32 s30, 0x200000, s28\n"
    "s_lshl_b32 s31, s83, 21\n"
    "s_add_u32 s31, s31, s28\n"
    "s_add_u32 s52, s12, s31\n"
    "s_addc_u32 s53, s13, 0\n"
    "s_mov_b32 s54, s30\n"
    "s_mov_b32 s55, 0x00020000\n"
    "s_mov_b32 s31, s28\n"
    "s_lshl_b32 s29, s83, 5\n"
    "s_add_u32 s31, s31, s29\n"
    "s_lshl_b32 s29, s95, 18\n"
    "s_add_u32 s31, s31, s29\n"
    "s_lshl_b32 s29, s25, 16\n"
    "s_add_u32 s31, s31, s29\n"
    "s_sub_u32 s30, 0x100000, s31\n"
    "s_add_u32 s60, s14, s31\n"
    "s_addc_u32 s61, s15, 0\n"
    "s_mov_b32 s62, s30\n"
    "s_mov_b32 s63, 0x00020000\n"
    "s_mov_b32 s65, 0\n"
    "s_waitcnt vmcnt(19)\n"
    "s_barrier\n"
    "ds_read_b128 v[48:51], v96 offset:0\n"
    "ds_read_b64_tr_b16 v[64:65], v100 offset:0\n"
    "ds_read_b64_tr_b16 v[66:67], v101 offset:0\n"
    "ds_read_b64_tr_b16 v[68:69], v102 offset:0\n"
    "ds_read_b64_tr_b16 v[70:71], v103 offset:0\n"
    "ds_read_b128 v[52:55], v97 offset:0\n"
    "ds_read_b64_tr_b16 v[72:73], v100 offset:4096\n"
    "ds_read_b64_tr_b16 v[74:75], v101 offset:4096\n"
    "ds_read_b64_tr_b16 v[76:77], v102 offset:4096\n"
    "ds_read_b64_tr_b16 v[78:79], v103 offset:4096\n"
    "ds_read_b128 v[56:59], v98 offset:0\n"
    "ds_read_b64_tr_b16 v[80:81], v100 offset:8192\n"
    "ds_read_b64_tr_b16 v[82:83], v101 offset:8192\n"
    "ds_read_b64_tr_b16 v[84:85], v102 offset:8192\n"
    "ds_read_b64_tr_b16 v[86:87], v103 offset:8192\n"
    "ds_read_b128 v[60:63], v99 offset:0\n"
    "ds_read_b64_tr_b16 v[88:89], v100 offset:12288\n"
    "ds_read_b64_tr_b16 v[90:91], v101 offset:12288\n"
    "ds_read_b64_tr_b16 v[92:93], v102 offset:12288\n"
    "ds_read_b64_tr_b16 v[94:95], v103 offset:12288\n"
    ".Lq_loop:\n"
    "s_waitcnt vmcnt(12) lgkmcnt(0)\n"
    "s_barrier\n"
    "v_mfma_f32_32x32x16_bf16 v[16:31], v[64:67], v[48:51], v[16:31]\n"
    "ds_read_b128 v[192:195], v96 offset:8192\n"
    "ds_read_b64_tr_b16 v[208:209], v100 offset:16384\n"
    "ds_read_b64_tr_b16 v[210:211], v101 offset:16384\n"
    "ds_read_b64_tr_b16 v[212:213], v102 offset:16384\n"
    "ds_read_b64_tr_b16 v[214:215], v103 offset:16384\n"
    "v_mfma_f32_32x32x16_bf16 v[32:47], v[68:71], v[48:51], v[32:47]\n"
    "ds_read_b128 v[196:199], v97 offset:8192\n"
    "ds_read_b64_tr_b16 v[216:217], v100 offset:20480\n"
    "ds_read_b64_tr_b16 v[218:219], v101 offset:20480\n"
    "ds_read_b64_tr_b16 v[220:221], v102 offset:20480\n"
    "ds_read_b64_tr_b16 v[222:223], v103 offset:20480\n"
    "v_mfma_f32_32x32x16_bf16 v[16:31], v[72:75], v[52:55], v[16:31]\n"
    "ds_read_b128 v[200:203], v98 offset:8192\n"
    "ds_read_b64_tr_b16 v[224:225], v100 offset:24576\n"
    "ds_read_b64_tr_b16 v[226:227], v101 offset:24576\n"
    "ds_read_b64_tr_b16 v[228:229], v102 offset:24576\n"
    "ds_read_b64_tr_b16 v[230:231], v103 offset:24576\n"
    "v_mfma_f32_32x32x16_bf16 v[32:47], v[76:79], v[52:55], v[32:47]\n"
    "ds_read_b128 v[204:207], v99 offset:8192\n"
    "ds_read_b64_tr_b16 v[232:233], v100 offset:28672\n"
    "ds_read_b64_tr_b16 v[234:235], v101 offset:28672\n"
    "ds_read_b64_tr_b16 v[236:237], v102 offset:28672\n"
    "ds_read_b64_tr_b16 v[238:239], v103 offset:28672\n"
    "s_add_u32 m0, s81, 32768\n"
    "v_mfma_f32_32x32x16_bf16 v[16:31], v[80:83], v[56:59], v[16:31]\n"
    "buffer_load_dwordx4 v161, s[36:39], s73 offen offset:0 lds\n"
    "buffer_load_dwordx4 v162, s[36:39], s73 offen offset:1024 lds\n"
    "v_mfma_f32_32x32x16_bf16 v[32:47], v[84:87], v[56:59], v[32:47]\n"
    "buffer_load_dwordx4 v163, s[36:39], s73 offen offset:2048 lds\n"
    "buffer_load_dwordx4 v164, s[36:39], s73 offen offset:3072 lds\n"
    "v_mfma_f32_32x32x16_bf16 v[16:31], v[88:91], v[60:63], v[16:31]\n"
    "s_add_u32 m0, s80, 0\n"
    "s_add_u32 s73, s73, 0x80000\n"
    "buffer_load_dwordx4 v108, s[20:23], s72 offen offset:0 lds\n"
    "v_mfma_f32_32x32x16_bf16 v[32:47], v[92:95], v[60:63], v[32:47]\n"
    "buffer_load_dwordx4 v109, s[20:23], s72 offen offset:1024 lds\n"
    "s_add_u32 s72, s72, 128\n"
    "s_and_b32 s73, s73, 0x7fffff\n"
    "s_and_b32 s72, s72, 0x7ff\n"
    "s_waitcnt vmcnt(12) lgkmcnt(0)\n"
    "s_barrier\n"
    "v_mfma_f32_32x32x16_bf16 v[16:31], v[208:211], v[192:195], v[16:31]\n"
    "ds_read_b128 v[48:51], v96 offset:16384\n"
    "ds_read_b64_tr_b16 v[64:65], v104 offset:0\n"
    "ds_read_b64_tr_b16 v[66:67], v105 offset:0\n"
    "ds_read_b64_tr_b16 v[68:69], v106 offset:0\n"
    "ds_read_b64_tr_b16 v[70:71], v107 offset:0\n"
    "v_mfma_f32_32x32x16_bf16 v[32:47], v[212:215], v[192:195], v[32:47]\n"
    "ds_read_b128 v[52:55], v97 offset:16384\n"
    "ds_read_b64_tr_b16 v[72:73], v104 offset:4096\n"
    "ds_read_b64_tr_b16 v[74:75], v105 offset:4096\n"
    "ds_read_b64_tr_b16 v[76:77], v106 offset:4096\n"
    "ds_read_b64_tr_b16 v[78:79], v107 offset:4096\n"
    "v_mfma_f32_32x32x16_bf16 v[16:31], v[216:219], v[196:199], v[16:31]\n"
    "ds_read_b128 v[56:59], v98 offset:16384\n"
    "ds_read_b64_tr_b16 v[80:81], v104 offset:8192\n"
    "ds_read_b64_tr_b16 v[82:83], v105 offset:8192\n"
    "ds_read_b64_tr_b16 v[84:85], v106 offset:8192\n"
    "ds_read_b64_tr_b16 v[86:87], v107 offset:8192\n"
    "v_mfma_f32_32x32x16_bf16 v[32:47], v[220:223], v[196:199], v[32:47]\n"
    "ds_read_b128 v[60:63], v99 offset:16384\n"
    "ds_read_b64_tr_b16 v[88:89], v104 offset:12288\n"
    "ds_read_b64_tr_b16 v[90:91], v105 offset:12288\n"
    "ds_read_b64_tr_b16 v[92:93], v106 offset:12288\n"
    "ds_read_b64_tr_b16 v[94:95], v107 offset:12288\n"
    "s_add_u32 m0, s81, 49152\n"
    "v_mfma_f32_32x32x16_bf16 v[16:31], v[224:227], v[200:203], v[16:31]\n"
    "buffer_load_dwordx4 v161, s[36:39], s73 offen offset:0 lds\n"
    "buffer_load_dwordx4 v162, s[36:39], s73 offen offset:1024 lds\n"
    "v_mfma_f32_32x32x16_bf16 v[32:47], v[228:231], v[200:203], v[32:47]\n"
    "buffer_load_dwordx4 v163, s[36:39], s73 offen offset:2048 lds\n"
    "buffer_load_dwordx4 v164, s[36:39], s73 offen offset:3072 lds\n"
    "v_mfma_f32_32x32x16_bf16 v[16:31], v[232:235], v[204:207], v[16:31]\n"
    "s_add_u32 m0, s80, 8192\n"
    "s_add_u32 s73, s73, 0x80000\n"
    "buffer_load_dwordx4 v108, s[20:23], s72 offen offset:0 lds\n"
    "v_mfma_f32_32x32x16_bf16 v[32:47], v[236:239], v[204:207], v[32:47]\n"
    "buffer_load_dwordx4 v109, s[20:23], s72 offen offset:1024 lds\n"
    "s_add_u32 s72, s72, 128\n"
    "s_and_b32 s73, s73, 0x7fffff\n"
    "s_and_b32 s72, s72, 0x7ff\n"
    "s_waitcnt vmcnt(12) lgkmcnt(0)\n"
    "s_barrier\n"
    "v_mfma_f32_32x32x16_bf16 v[16:31], v[64:67], v[48:51], v[16:31]\n"
    "ds_read_b128 v[192:195], v96 offset:24576\n"
    "ds_read_b64_tr_b16 v[208:209], v104 offset:16384\n"
    "ds_read_b64_tr_b16 v[210:211], v105 offset:16384\n"
    "ds_read_b64_tr_b16 v[212:213], v106 offset:16384\n"
    "ds_read_b64_tr_b16 v[214:215], v107 offset:16384\n"
    "v_mfma_f32_32x32x16_bf16 v[32:47], v[68:71], v[48:51], v[32:47]\n"
    "ds_read_b128 v[196:199], v97 offset:24576\n"
    "ds_read_b64_tr_b16 v[216:217], v104 offset:20480\n"
    "ds_read_b64_tr_b16 v[218:219], v105 offset:20480\n"
    "ds_read_b64_tr_b16 v[220:221], v106 offset:20480\n"
    "ds_read_b64_tr_b16 v[222:223], v107 offset:20480\n"
    "v_mfma_f32_32x32x16_bf16 v[16:31], v[72:75], v[52:55], v[16:31]\n"
    "ds_read_b128 v[200:203], v98 offset:24576\n"
    "ds_read_b64_tr_b16 v[224:225], v104 offset:24576\n"
    "ds_read_b64_tr_b16 v[226:227], v105 offset:24576\n"
    "ds_read_b64_tr_b16 v[228:229], v106 offset:24576\n"
    "ds_read_b64_tr_b16 v[230:231], v107 offset:24576\n"
    "v_mfma_f32_32x32x16_bf16 v[32:47], v[76:79], v[52:55], v[32:47]\n"
    "ds_read_b128 v[204:207], v99 offset:24576\n"
    "ds_read_b64_tr_b16 v[232:233], v104 offset:28672\n"
    "ds_read_b64_tr_b16 v[234:235], v105 offset:28672\n"
    "ds_read_b64_tr_b16 v[236:237], v106 offset:28672\n"
    "ds_read_b64_tr_b16 v[238:239], v107 offset:28672\n"
    "s_add_u32 m0, s81, 98304\n"
    "v_mfma_f32_32x32x16_bf16 v[16:31], v[80:83], v[56:59], v[16:31]\n"
    "buffer_load_dwordx4 v161, s[36:39], s73 offen offset:0 lds\n"
    "buffer_load_dwordx4 v162, s[36:39], s73 offen offset:1024 lds\n"
    "v_mfma_f32_32x32x16_bf16 v[32:47], v[84:87], v[56:59], v[32:47]\n"
    "buffer_load_dwordx4 v163, s[36:39], s73 offen offset:2048 lds\n"
    "buffer_load_dwordx4 v164, s[36:39], s73 offen offset:3072 lds\n"
    "v_mfma_f32_32x32x16_bf16 v[16:31], v[88:91], v[60:63], v[16:31]\n"
    "s_add_u32 m0, s80, 16384\n"
    "s_add_u32 s73, s73, 0x80000\n"
    "buffer_load_dwordx4 v108, s[20:23], s72 offen offset:0 lds\n"
    "v_mfma_f32_32x32x16_bf16 v[32:47], v[92:95], v[60:63], v[32:47]\n"
    "buffer_load_dwordx4 v109, s[20:23], s72 offen offset:1024 lds\n"
    "s_add_u32 s72, s72, 128\n"
    "s_and_b32 s73, s73, 0x7fffff\n"
    "s_and_b32 s72, s72, 0x7ff\n"
    "s_waitcnt vmcnt(12) lgkmcnt(0)\n"
    "s_barrier\n"
    "v_mfma_f32_32x32x16_bf16 v[16:31], v[208:211], v[192:195], v[16:31]\n"
    "ds_read_b128 v[48:51], v96 offset:0\n"
    "ds_read_b64_tr_b16 v[64:65], v100 offset:0\n"
    "ds_read_b64_tr_b16 v[66:67], v101 offset:0\n"
    "ds_read_b64_tr_b16 v[68:69], v102 offset:0\n"
    "ds_read_b64_tr_b16 v[70:71], v103 offset:0\n"
    "v_mfma_f32_32x32x16_bf16 v[32:47], v[212:215], v[192:195], v[32:47]\n"
    "ds_read_b128 v[52:55], v97 offset:0\n"
    "ds_read_b64_tr_b16 v[72:73], v100 offset:4096\n"
    "ds_read_b64_tr_b16 v[74:75], v101 offset:4096\n"
    "ds_read_b64_tr_b16 v[76:77], v102 offset:4096\n"
    "ds_read_b64_tr_b16 v[78:79], v103 offset:4096\n"
    "v_mfma_f32_32x32x16_bf16 v[16:31], v[216:219], v[196:199], v[16:31]\n"
    "ds_read_b128 v[56:59], v98 offset:0\n"
    "ds_read_b64_tr_b16 v[80:81], v100 offset:8192\n"
    "ds_read_b64_tr_b16 v[82:83], v101 offset:8192\n"
    "ds_read_b64_tr_b16 v[84:85], v102 offset:8192\n"
    "ds_read_b64_tr_b16 v[86:87], v103 offset:8192\n"
    "v_mfma_f32_32x32x16_bf16 v[32:47], v[220:223], v[196:199], v[32:47]\n"
    "ds_read_b128 v[60:63], v99 offset:0\n"
    "ds_read_b64_tr_b16 v[88:89], v100 offset:12288\n"
    "ds_read_b64_tr_b16 v[90:91], v101 offset:12288\n"
    "ds_read_b64_tr_b16 v[92:93], v102 offset:12288\n"
    "ds_read_b64_tr_b16 v[94:95], v103 offset:12288\n"
    "s_add_u32 m0, s81, 114688\n"
    "v_mfma_f32_32x32x16_bf16 v[16:31], v[224:227], v[200:203], v[16:31]\n"
    "buffer_load_dwordx4 v161, s[36:39], s73 offen offset:0 lds\n"
    "buffer_load_dwordx4 v162, s[36:39], s73 offen offset:1024 lds\n"
    "v_mfma_f32_32x32x16_bf16 v[32:47], v[228:231], v[200:203], v[32:47]\n"
    "buffer_load_dwordx4 v163, s[36:39], s73 offen offset:2048 lds\n"
    "buffer_load_dwordx4 v164, s[36:39], s73 offen offset:3072 lds\n"
    "v_mfma_f32_32x32x16_bf16 v[16:31], v[232:235], v[204:207], v[16:31]\n"
    "s_add_u32 m0, s80, 24576\n"
    "s_add_u32 s73, s73, 0x80000\n"
    "buffer_load_dwordx4 v108, s[20:23], s72 offen offset:0 lds\n"
    "v_mfma_f32_32x32x16_bf16 v[32:47], v[236:239], v[204:207], v[32:47]\n"
    "buffer_load_dwordx4 v109, s[20:23], s72 offen offset:1024 lds\n"
    "s_add_u32 s72, s72, 128\n"
    "s_and_b32 s73, s73, 0x7fffff\n"
    "s_and_b32 s72, s72, 0x7ff\n"
    "s_add_u32 s65, s65, 1\n"
    "s_cmp_lt_u32 s65, 3\n"
    "s_cbranch_scc1 .Lq_loop\n"
    "s_waitcnt vmcnt(12) lgkmcnt(0)\n"
    "s_barrier\n"
    "v_mfma_f32_32x32x16_bf16 v[16:31], v[64:67], v[48:51], v[16:31]\n"
    "ds_read_b128 v[192:195], v96 offset:8192\n"
    "ds_read_b64_tr_b16 v[208:209], v100 offset:16384\n"
    "ds_read_b64_tr_b16 v[210:211], v101 offset:16384\n"
    "ds_read_b64_tr_b16 v[212:213], v102 offset:16384\n"
    "ds_read_b64_tr_b16 v[214:215], v103 offset:16384\n"
    "v_mfma_f32_32x32x16_bf16 v[32:47], v[68:71], v[48:51], v[32:47]\n"
    "ds_read_b128 v[196:199], v97 offset:8192\n"
    "ds_read_b64_tr_b16 v[216:217], v100 offset:20480\n"
    "ds_read_b64_tr_b16 v[218:219], v101 offset:20480\n"
    "ds_read_b64_tr_b16 v[220:221], v102 offset:20480\n"
    "ds_read_b64_tr_b16 v[222:223], v103 offset:20480\n"
    "v_mfma_f32_32x32x16_bf16 v[16:31], v[72:75], v[52:55], v[16:31]\n"
    "ds_read_b128 v[200:203], v98 offset:8192\n"
    "ds_read_b64_tr_b16 v[224:225], v100 offset:24576\n"
    "ds_read_b64_tr_b16 v[226:227], v101 offset:24576\n"
    "ds_read_b64_tr_b16 v[228:229], v102 offset:24576\n"
    "ds_read_b64_tr_b16 v[230:231], v103 offset:24576\n"
    "v_mfma_f32_32x32x16_bf16 v[32:47], v[76:79], v[52:55], v[32:47]\n"
    "ds_read_b128 v[204:207], v99 offset:8192\n"
    "ds_read_b64_tr_b16 v[232:233], v100 offset:28672\n"
    "ds_read_b64_tr_b16 v[234:235], v101 offset:28672\n"
    "ds_read_b64_tr_b16 v[236:237], v102 offset:28672\n"
    "ds_read_b64_tr_b16 v[238:239], v103 offset:28672\n"
    "v_mfma_f32_32x32x16_bf16 v[16:31], v[80:83], v[56:59], v[16:31]\n"
    "v_mfma_f32_32x32x16_bf16 v[32:47], v[84:87], v[56:59], v[32:47]\n"
    "v_mfma_f32_32x32x16_bf16 v[16:31], v[88:91], v[60:63], v[16:31]\n"
    "v_mfma_f32_32x32x16_bf16 v[32:47], v[92:95], v[60:63], v[32:47]\n"
    "s_waitcnt vmcnt(6) lgkmcnt(0)\n"
    "s_barrier\n"
    "v_mfma_f32_32x32x16_bf16 v[16:31], v[208:211], v[192:195], v[16:31]\n"
    "ds_read_b128 v[48:51], v96 offset:16384\n"
    "ds_read_b64_tr_b16 v[64:65], v104 offset:0\n"
    "ds_read_b64_tr_b16 v[66:67], v105 offset:0\n"
    "ds_read_b64_tr_b16 v[68:69], v106 offset:0\n"
    "ds_read_b64_tr_b16 v[70:71], v107 offset:0\n"
    "v_mfma_f32_32x32x16_bf16 v[32:47], v[212:215], v[192:195], v[32:47]\n"
    "ds_read_b128 v[52:55], v97 offset:16384\n"
    "ds_read_b64_tr_b16 v[72:73], v104 offset:4096\n"
    "ds_read_b64_tr_b16 v[74:75], v105 offset:4096\n"
    "ds_read_b64_tr_b16 v[76:77], v106 offset:4096\n"
    "ds_read_b64_tr_b16 v[78:79], v107 offset:4096\n"
    "v_mfma_f32_32x32x16_bf16 v[16:31], v[216:219], v[196:199], v[16:31]\n"
    "ds_read_b128 v[56:59], v98 offset:16384\n"
    "ds_read_b64_tr_b16 v[80:81], v104 offset:8192\n"
    "ds_read_b64_tr_b16 v[82:83], v105 offset:8192\n"
    "ds_read_b64_tr_b16 v[84:85], v106 offset:8192\n"
    "ds_read_b64_tr_b16 v[86:87], v107 offset:8192\n"
    "v_mfma_f32_32x32x16_bf16 v[32:47], v[220:223], v[196:199], v[32:47]\n"
    "ds_read_b128 v[60:63], v99 offset:16384\n"
    "ds_read_b64_tr_b16 v[88:89], v104 offset:12288\n"
    "ds_read_b64_tr_b16 v[90:91], v105 offset:12288\n"
    "ds_read_b64_tr_b16 v[92:93], v106 offset:12288\n"
    "ds_read_b64_tr_b16 v[94:95], v107 offset:12288\n"
    "v_mfma_f32_32x32x16_bf16 v[16:31], v[224:227], v[200:203], v[16:31]\n"
    "v_mfma_f32_32x32x16_bf16 v[32:47], v[228:231], v[200:203], v[32:47]\n"
    "v_mfma_f32_32x32x16_bf16 v[16:31], v[232:235], v[204:207], v[16:31]\n"
    "v_mfma_f32_32x32x16_bf16 v[32:47], v[236:239], v[204:207], v[32:47]\n"
    "s_cmp_lg_u32 s18, 0\n"
    "s_cbranch_scc1 .Lno_early3\n"
    "s_add_u32 m0, s71, 0\n"
    "s_nop 0\n"
    "buffer_load_dwordx4 v124, s[40:43], s66 offen offset:0 nt lds\n"
    "buffer_load_dwordx4 v125, s[40:43], s66 offen offset:1024 nt lds\n"
    "buffer_load_dwordx4 v126, s[40:43], s66 offen offset:2048 nt lds\n"
    "buffer_load_dwordx4 v127, s[40:43], s66 offen offset:3072 nt lds\n"
    "s_add_u32 s66, s66, 16384\n"
    "s_add_u32 m0, s71, 8192\n"
    "s_nop 0\n"
    "buffer_load_dwordx4 v128, s[44:47], s67 offen offset:0 nt lds\n"
    "buffer_load_dwordx4 v129, s[44:47], s67 offen offset:1024 nt lds\n"
    "buffer_load_dwordx4 v128, s[44:47], s67 offen offset:2048 nt lds\n"
    "buffer_load_dwordx4 v129, s[44:47], s67 offen offset:3072 nt lds\n"
    "s_add_u32 s67, s67, 16384\n"
    "s_add_u32 m0, s71, 16384\n"
    "s_nop 0\n"
    "buffer_load_dwordx4 v124, s[40:43], s66 offen offset:0 nt lds\n"
    "buffer_load_dwordx4 v125, s[40:43], s66 offen offset:1024 nt lds\n"
    "buffer_load_dwordx4 v126, s[40:43], s66 offen offset:2048 nt lds\n"
    "buffer_load_dwordx4 v127, s[40:43], s66 offen offset:3072 nt lds\n"
    "s_add_u32 s66, s66, 16384\n"
    "s_add_u32 m0, s71, 24576\n"
    "s_nop 0\n"
    "buffer_load_dwordx4 v128, s[44:47], s67 offen offset:0 nt lds\n"
    "buffer_load_dwordx4 v129, s[44:47], s67 offen offset:1024 nt lds\n"
    "buffer_load_dwordx4 v128, s[44:47], s67 offen offset:2048 nt lds\n"
    "buffer_load_dwordx4 v129, s[44:47], s67 offen offset:3072 nt lds\n"
    "s_add_u32 s67, s67, 16384\n"
    ".Lno_early3:\n"
    "s_cmp_eq_u32 s18, 0\n"
    "s_cbranch_scc1 .Lqa2\n"
    "s_waitcnt vmcnt(0)\n"
    ".Lqa2:\n"
    "s_waitcnt vmcnt(16) lgkmcnt(0)\n"
    "s_barrier\n"
    "v_mfma_f32_32x32x16_bf16 v[16:31], v[64:67], v[48:51], v[16:31]\n"
    "ds_read_b128 v[192:195], v96 offset:24576\n"
    "ds_read_b64_tr_b16 v[208:209], v104 offset:16384\n"
    "ds_read_b64_tr_b16 v[210:211], v105 offset:16384\n"
    "ds_read_b64_tr_b16 v[212:213], v106 offset:16384\n"
    "ds_read_b64_tr_b16 v[214:215], v107 offset:16384\n"
    "v_mfma_f32_32x32x16_bf16 v[32:47], v[68:71], v[48:51], v[32:47]\n"
    "ds_read_b128 v[196:199], v97 offset:24576\n"
    "ds_read_b64_tr_b16 v[216:217], v104 offset:20480\n"
    "ds_read_b64_tr_b16 v[218:219], v105 offset:20480\n"
    "ds_read_b64_tr_b16 v[220:221], v106 offset:20480\n"
    "ds_read_b64_tr_b16 v[222:223], v107 offset:20480\n"
    "v_mfma_f32_32x32x16_bf16 v[16:31], v[72:75], v[52:55], v[16:31]\n"
    "ds_read_b128 v[200:203], v98 offset:24576\n"
    "ds_read_b64_tr_b16 v[224:225], v104 offset:24576\n"
    "ds_read_b64_tr_b16 v[226:227], v105 offset:24576\n"
    "ds_read_b64_tr_b16 v[228:229], v106 offset:24576\n"
    "ds_read_b64_tr_b16 v[230:231], v107 offset:24576\n"
    "v_mfma_f32_32x32x16_bf16 v[32:47], v[76:79], v[52:55], v[32:47]\n"
    "ds_read_b128 v[204:207], v99 offset:24576\n"
    "ds_read_b64_tr_b16 v[232:233], v104 offset:28672\n"
    "ds_read_b64_tr_b16 v[234:235], v105 offset:28672\n"
    "ds_read_b64_tr_b16 v[236:237], v106 offset:28672\n"
    "ds_read_b64_tr_b16 v[238:239], v107 offset:28672\n"
    "v_mfma_f32_32x32x16_bf16 v[16:31], v[80:83], v[56:59], v[16:31]\n"
    "v_mfma_f32_32x32x16_bf16 v[32:47], v[84:87], v[56:59], v[32:47]\n"
    "v_mfma_f32_32x32x16_bf16 v[16:31], v[88:91], v[60:63], v[16:31]\n"
    "v_mfma_f32_32x32x16_bf16 v[32:47], v[92:95], v[60:63], v[32:47]\n"
    "s_waitcnt lgkmcnt(0)\n"
    "v_mfma_f32_32x32x16_bf16 v[16:31], v[208:211], v[192:195], v[16:31]\n"
    "v_mfma_f32_32x32x16_bf16 v[32:47], v[212:215], v[192:195], v[32:47]\n"
    "v_mfma_f32_32x32x16_bf16 v[16:31], v[216:219], v[196:199], v[16:31]\n"
    "v_mfma_f32_32x32x16_bf16 v[32:47], v[220:223], v[196:199], v[32:47]\n"
    "v_mfma_f32_32x32x16_bf16 v[16:31], v[224:227], v[200:203], v[16:31]\n"
    "v_mfma_f32_32x32x16_bf16 v[32:47], v[228:231], v[200:203], v[32:47]\n"
    "v_mfma_f32_32x32x16_bf16 v[16:31], v[232:235], v[204:207], v[16:31]\n"
    "v_mfma_f32_32x32x16_bf16 v[32:47], v[236:239], v[204:207], v[32:47]\n"
    "v_readfirstlane_b32 s64, v154\n"
    "s_lshr_b32 s28, s27, 2\n"
    "s_lshl_b32 s28, s28, 19\n"
    "s_lshl_b32 s29, s19, 18\n"
    "s_add_u32 s28, s28, s29\n"
    "s_lshl_b32 s29, s26, 14\n"
    "s_add_u32 s28, s28, s29\n"
    "s_lshl_b32 s29, s93, 12\n"
    "s_add_u32 s28, s28, s29\n"
    "s_lshl_b32 s29, s82, 11\n"
    "s_add_u32 s28, s28, s29\n"
    "s_add_u32 s28, s28, 1048576\n"
    "s_add_u32 s56, s28, 1024\n"
    "s_add_u32 s64, s64, 1\n"
    "v_lshlrev_b32 v1, 4, v149\n"
    "v_lshl_add_u32 v1, v158, 8, v1\n"
    "v_lshl_add_u32 v2, v159, 17, v1\n"
    "s_mov_b32 s30, 0x3e0293ee\n"
    "v_pk_mul_f32 v[16:17], v[16:17], s[30:31] op_sel_hi:[1,0]\n"
    "v_pk_mul_f32 v[18:19], v[18:19], s[30:31] op_sel_hi:[1,0]\n"
    "v_pk_mul_f32 v[20:21], v[20:21], s[30:31] op_sel_hi:[1,0]\n"
    "v_pk_mul_f32 v[22:23], v[22:23], s[30:31] op_sel_hi:[1,0]\n"
    "v_cvt_pk_bf16_f32 v48, v16, v17\n"
    "v_cvt_pk_bf16_f32 v49, v18, v19\n"
    "v_cvt_pk_bf16_f32 v50, v20, v21\n"
    "v_cvt_pk_bf16_f32 v51, v22, v23\n"
    "buffer_store_dwordx4 v[48:51], v2, s[48:51], s28 offen offset:0 sc1\n"
    "v_pk_mul_f32 v[24:25], v[24:25], s[30:31] op_sel_hi:[1,0]\n"
    "v_pk_mul_f32 v[26:27], v[26:27], s[30:31] op_sel_hi:[1,0]\n"
    "v_pk_mul_f32 v[28:29], v[28:29], s[30:31] op_sel_hi:[1,0]\n"
    "v_pk_mul_f32 v[30:31], v[30:31], s[30:31] op_sel_hi:[1,0]\n"
    "v_cvt_pk_bf16_f32 v52, v24, v25\n"
    "v_cvt_pk_bf16_f32 v53, v26, v27\n"
    "v_cvt_pk_bf16_f32 v54, v28, v29\n"
    "v_cvt_pk_bf16_f32 v55, v30, v31\n"
    "buffer_store_dwordx4 v[52:55], v2, s[48:51], s28 offen offset:512 sc1\n"
    "v_pk_mul_f32 v[32:33], v[32:33], s[30:31] op_sel_hi:[1,0]\n"
    "v_pk_mul_f32 v[34:35], v[34:35], s[30:31] op_sel_hi:[1,0]\n"
    "v_pk_mul_f32 v[36:37], v[36:37], s[30:31] op_sel_hi:[1,0]\n"
    "v_pk_mul_f32 v[38:39], v[38:39], s[30:31] op_sel_hi:[1,0]\n"
    "v_cvt_pk_bf16_f32 v56, v32, v33\n"
    "v_cvt_pk_bf16_f32 v57, v34, v35\n"
    "v_cvt_pk_bf16_f32 v58, v36, v37\n"
    "v_cvt_pk_bf16_f32 v59, v38, v39\n"
    "buffer_store_dwordx4 v[56:59], v2, s[48:51], s56 offen offset:0 sc1\n"
    "v_pk_mul_f32 v[40:41], v[40:41], s[30:31] op_sel_hi:[1,0]\n"
    "v_pk_mul_f32 v[42:43], v[42:43], s[30:31] op_sel_hi:[1,0]\n"
    "v_pk_mul_f32 v[44:45], v[44:45], s[30:31] op_sel_hi:[1,0]\n"
    "v_pk_mul_f32 v[46:47], v[46:47], s[30:31] op_sel_hi:[1,0]\n"
    "v_cvt_pk_bf16_f32 v60, v40, v41\n"
    "v_cvt_pk_bf16_f32 v61, v42, v43\n"
    "v_cvt_pk_bf16_f32 v62, v44, v45\n"
    "v_cvt_pk_bf16_f32 v63, v46, v47\n"
    "buffer_store_dwordx4 v[60:63], v2, s[48:51], s56 offen offset:512 sc1\n"
    "s_lshl_b32 s28, s24, 8\n"
    "s_lshl_b32 s29, s25, 2\n"
    "s_add_u32 s28, s28, s29\n"
    "s_add_u32 s28, s28, 4096\n"
    "v_mov_b32 v3, s64\n"
    "v_mov_b32 v1, 0\n"
    "s_and_b32 s31, s96, 3\n"
    "s_lshl_b32 s31, s31, 9\n"
    "s_lshl_b32 s29, s19, 8\n"
    "s_add_u32 s31, s31, s29\n"
    "s_lshr_b32 s29, s96, 4\n"
    "s_lshl_b32 s29, s29, 13\n"
    "s_add_u32 s31, s31, s29\n"
    "s_add_u32 s31, s31, 4096\n"
    "v_lshlrev_b32 v4, 11, v151\n"
    "v_lshl_add_u32 v155, v152, 2, v4\n"
    "s_mov_b32 s76, 0x49800000\n"
    "s_mul_i32 s77, s18, -36\n"
    "s_add_u32 s77, s77, 126\n"
    "s_mul_i32 s78, s18, 1\n"
    "s_mul_i32 s65, s18, 2\n"
    "s_add_u32 s65, s65, 1\n"
    "s_lshl_b32 s30, s96, 15\n"
    "s_lshl_b32 s29, s19, 14\n"
    "s_add_u32 s30, s30, s29\n"
    "s_add_u32 s30, s30, 1048576\n"
    "s_waitcnt vmcnt(0)\n"
    "s_mov_b64 exec, 1\n"
    "buffer_store_dword v3, v1, s[48:51], s28 offen sc1\n"
    "s_mov_b64 exec, -1\n"
    "s_barrier\n"
    "ds_read_b128 a[192:195], v112 offset:0\n"
    "ds_read_b128 a[208:211], v112 offset:4096\n"
    "ds_read_b128 a[196:199], v113 offset:0\n"
    "ds_read_b128 a[212:215], v113 offset:4096\n"
    "ds_read_b128 a[200:203], v114 offset:0\n"
    "ds_read_b128 a[216:219], v114 offset:4096\n"
    "ds_read_b128 a[204:207], v115 offset:0\n"
    "ds_read_b128 a[220:223], v115 offset:4096\n"
    "s_add_u32 m0, s71, 32768\n"
    "s_nop 0\n"
    "buffer_load_dwordx4 v124, s[40:43], s66 offen offset:0 nt lds\n"
    "buffer_load_dwordx4 v125, s[40:43], s66 offen offset:1024 nt lds\n"
    "buffer_load_dwordx4 v126, s[40:43], s66 offen offset:2048 nt lds\n"
    "buffer_load_dwordx4 v127, s[40:43], s66 offen offset:3072 nt lds\n"
    "s_add_u32 s66, s66, 16384\n"
    "s_add_u32 m0, s71, 49152\n"
    "s_nop 0\n"
    "buffer_load_dwordx4 v124, s[40:43], s66 offen offset:0 nt lds\n"
    "buffer_load_dwordx4 v125, s[40:43], s66 offen offset:1024 nt lds\n"
    "buffer_load_dwordx4 v126, s[40:43], s66 offen offset:2048 nt lds\n"
    "buffer_load_dwordx4 v127, s[40:43], s66 offen offset:3072 nt lds\n"
    "s_add_u32 s66, s66, 16384\n"
    "s_lshl_b32 s28, s96, 2\n"
    "s_add_u32 s28, s28, s25\n"
    "s_lshl_b32 s4, s28, 13\n"
    "s_add_u32 s4, s4, 13631488\n"
    "s_add_u32 s5, s4, 4096\n"
    "s_lshl_b32 s6, s28, 10\n"
    "s_add_u32 s6, s6, 524288\n"
    "s_xor_b32 s7, s24, 2\n"
    "s_lshl_b32 s7, s7, 14\n"
    "s_lshl_b32 s29, s25, 12\n"
    "s_add_u32 s7, s7, s29\n"
    "s_add_u32 s7, s7, 9437184\n"
    "s_xor_b32 s8, s24, 4\n"
    "s_lshl_b32 s8, s8, 14\n"
    "s_lshl_b32 s29, s25, 12\n"
    "s_add_u32 s8, s8, s29\n"
    "s_add_u32 s8, s8, 9437184\n"
    "s_xor_b32 s9, s24, 6\n"
    "s_lshl_b32 s9, s9, 14\n"
    "s_lshl_b32 s29, s25, 12\n"
    "s_add_u32 s9, s9, s29\n"
    "s_add_u32 s9, s9, 9437184\n"
    "s_lshl_b32 s12, s24, 14\n"
    "s_lshl_b32 s29, s25, 12\n"
    "s_add_u32 s12, s12, s29\n"
    "s_add_u32 s12, s12, 9437184\n"
    "s_lshl_b32 s10, s96, 15\n"
    "s_lshl_b32 s29, s19, 14\n"
    "s_add_u32 s10, s10, s29\n"
    "s_lshl_b32 s29, s82, 11\n"
    "s_add_u32 s10, s10, s29\n"
    "s_add_u32 s10, s10, 5242880\n"
    "s_lshl_b32 s11, s24, 8\n"
    "s_lshl_b32 s29, s25, 2\n"
    "s_add_u32 s11, s11, s29\n"
    "s_add_u32 s11, s11, 4224\n"
    "s_andn2_b32 s28, s24, 8\n"
    "s_lshl_b32 s13, s28, 8\n"
    "s_lshl_b32 s29, s25, 2\n"
    "s_add_u32 s13, s13, s29\n"
    "s_add_u32 s13, s13, 4224\n"
    "s_lshl_b32 s14, s95, 20\n"
    "s_lshl_b32 s29, s25, 18\n"
    "s_add_u32 s14, s14, s29\n"
    "s_lshl_b32 s29, s83, 15\n"
    "s_add_u32 s14, s14, s29\n"
    "s_add_u32 s14, s14, 5242880\n"
    "s_add_u32 s15, s14, 0x20000\n"
    "v_lshlrev_b32 v14, 5, v149\n"
    "s_lshl_b32 s28, s25, 13\n"
    "s_add_u32 s28, s28, 131072\n"
    "v_add_u32 v161, s28, v153\n"
    "v_xor_b32 v163, 0x4000, v161\n"
    "s_add_u32 s56, s19, 2\n"
    "s_lshl_b32 s28, s82, 16\n"
    "s_and_b32 s29, s56, 3\n"
    "s_lshl_b32 s29, s29, 14\n"
    "s_add_u32 s28, s28, s29\n"
    "s_add_u32 s28, s28, 8192\n"
    "v_add_u32 v162, s28, v153\n"
    "v_xor_b32 v164, 0x10000, v162\n"
    "s_add_u32 s28, s71, 27648\n"
    "v_add_u32 v165, s28, v14\n"
    "v_xor_b32 v166, 0x10000, v165\n"
    "v_lshrrev_b32 v5, 1, v151\n"
    "v_lshl_add_u32 v5, v159, 1, v5\n"
    "v_xor_b32 v5, s83, v5\n"
    "v_lshrrev_b32 v6, 1, v152\n"
    "v_lshl_add_u32 v5, v5, 1, v6\n"
    "v_lshl_add_u32 v6, v150, 2, v151\n"
    "v_and_b32 v4, 1, v152\n"
    "v_lshlrev_b32 v4, 3, v4\n"
    "v_lshl_add_u32 v6, v6, 7, v4\n"
    "v_lshl_add_u32 v154, v5, 4, v6\n"
    "s_lshl_b32 s28, s95, 9\n"
    "s_lshl_b32 s29, s82, 8\n"
    "s_add_u32 s28, s28, s29\n"
    "s_lshl_b32 s29, s19, 15\n"
    "s_add_u32 s28, s28, s29\n"
    "s_lshl_b32 s29, s83, 12\n"
    "s_add_u32 s28, s28, s29\n"
    "s_add_u32 s28, s28, 6272\n"
    "v_bfe_u32 v5, v148, 2, 1\n"
    "v_lshlrev_b32 v5, 14, v5\n"
    "v_lshl_add_u32 v5, v152, 2, v5\n"
    "v_add_u32 v5, s28, v5\n"
    "v_lshlrev_b32 v4, 1, v152\n"
    "v_xor_b32 v4, s24, v4\n"
    "s_lshl_b32 s28, s25, 2\n"
    "s_add_u32 s28, s28, 4224\n"
    "v_lshl_add_u32 v4, v4, 8, s28\n"
    "v_and_b32 v6, 8, v148\n"
    "v_cmp_eq_u32 vcc, 0, v6\n"
    "v_lshlrev_b32 v158, 11, v149\n"
    "v_lshl_add_u32 v158, v150, 3, v158\n"
    "v_cndmask_b32 v157, v4, v5, vcc\n"
    "v_mov_b32 v12, 0\n"
    "s_waitcnt lgkmcnt(0)\n"
    "s_barrier\n"
    "s_mov_b32 s79, 65536\n"
    ".Lq_poll:\n"
    "buffer_load_dword v3, v155, s[48:51], s31 offen sc1\n"
    "s_waitcnt vmcnt(0)\n"
    "v_cmp_ne_u32 vcc, s64, v3\n"
    "s_sub_u32 s79, s79, 1\n"
    "s_cbranch_vccz .Lq_done\n"
    "s_cbranch_scc0 .Lq_poll\n"
    ".Lq_done:\n"
    "buffer_load_dwordx4 a[128:131], v153, s[48:51], s30 offen offset:0 sc1\n"
    "buffer_load_dwordx4 a[132:135], v153, s[48:51], s30 offen offset:1024 sc1\n"
    "buffer_load_dwordx4 a[136:139], v153, s[48:51], s30 offen offset:2048 sc1\n"
    "buffer_load_dwordx4 a[140:143], v153, s[48:51], s30 offen offset:3072 sc1\n"
    "s_add_u32 s30, s30, 4096\n"
    "buffer_load_dwordx4 a[144:147], v153, s[48:51], s30 offen offset:0 sc1\n"
    "buffer_load_dwordx4 a[148:151], v153, s[48:51], s30 offen offset:1024 sc1\n"
    "buffer_load_dwordx4 a[152:155], v153, s[48:51], s30 offen offset:2048 sc1\n"
    "buffer_load_dwordx4 a[156:159], v153, s[48:51], s30 offen offset:3072 sc1\n"
    "s_add_u32 s30, s30, 4096\n"
    "buffer_load_dwordx4 a[160:163], v153, s[48:51], s30 offen offset:0 sc1\n"
    "buffer_load_dwordx4 a[164:167], v153, s[48:51], s30 offen offset:1024 sc1\n"
    "buffer_load_dwordx4 a[168:171], v153, s[48:51], s30 offen offset:2048 sc1\n"
    "buffer_load_dwordx4 a[172:175], v153, s[48:51], s30 offen offset:3072 sc1\n"
    "s_add_u32 s30, s30, 4096\n"
    "buffer_load_dwordx4 a[176:179], v153, s[48:51], s30 offen offset:0 sc1\n"
    "buffer_load_dwordx4 a[180:183], v153, s[48:51], s30 offen offset:1024 sc1\n"
    "buffer_load_dwordx4 a[184:187], v153, s[48:51], s30 offen offset:2048 sc1\n"
    "buffer_load_dwordx4 a[188:191], v153, s[48:51], s30 offen offset:3072 sc1\n"
    "s_add_u32 m0, s71, 40960\n"
    "s_nop 0\n"
    "buffer_load_dwordx4 v128, s[44:47], s67 offen offset:0 nt lds\n"
    "buffer_load_dwordx4 v129, s[44:47], s67 offen offset:1024 nt lds\n"
    "buffer_load_dwordx4 v128, s[44:47], s67 offen offset:2048 nt lds\n"
    "buffer_load_dwordx4 v129, s[44:47], s67 offen offset:3072 nt lds\n"
    "s_add_u32 s67, s67, 16384\n"
    "s_add_u32 m0, s71, 0\n"
    "s_nop 0\n"
    "buffer_load_dwordx4 v124, s[40:43], s66 offen offset:0 nt lds\n"
    "buffer_load_dwordx4 v125, s[40:43], s66 offen offset:1024 nt lds\n"
    "buffer_load_dwordx4 v126, s[40:43], s66 offen offset:2048 nt lds\n"
    "buffer_load_dwordx4 v127, s[40:43], s66 offen offset:3072 nt lds\n"
    "s_add_u32 s66, s66, 16384\n"
    "s_waitcnt vmcnt(8)\n"
    "v_mfma_f32_16x16x32_bf16 v[16:19], a[192:195], a[128:131], 0\n"
    "v_mfma_f32_16x16x32_bf16 v[24:27], a[192:195], a[144:147], 0\n"
    "v_mfma_f32_16x16x32_bf16 v[20:23], a[208:211], a[128:131], 0\n"
    "v_mfma_f32_16x16x32_bf16 v[28:31], a[208:211], a[144:147], 0\n"
    "v_mfma_f32_16x16x32_bf16 v[16:19], a[196:199], a[132:135], v[16:19]\n"
    "v_mfma_f32_16x16x32_bf16 v[24:27], a[196:199], a[148:151], v[24:27]\n"
    "v_mfma_f32_16x16x32_bf16 v[20:23], a[212:215], a[132:135], v[20:23]\n"
    "v_mfma_f32_16x16x32_bf16 v[28:31], a[212:215], a[148:151], v[28:31]\n"
    "v_mfma_f32_16x16x32_bf16 v[16:19], a[200:203], a[136:139], v[16:19]\n"
    "v_mfma_f32_16x16x32_bf16 v[24:27], a[200:203], a[152:155], v[24:27]\n"
    "v_mfma_f32_16x16x32_bf16 v[20:23], a[216:219], a[136:139], v[20:23]\n"
    "v_mfma_f32_16x16x32_bf16 v[28:31], a[216:219], a[152:155], v[28:31]\n"
    "v_mfma_f32_16x16x32_bf16 v[16:19], a[204:207], a[140:143], v[16:19]\n"
    "v_mfma_f32_16x16x32_bf16 v[24:27], a[204:207], a[156:159], v[24:27]\n"
    "v_mfma_f32_16x16x32_bf16 v[20:23], a[220:223], a[140:143], v[20:23]\n"
    "v_mfma_f32_16x16x32_bf16 v[28:31], a[220:223], a[156:159], v[28:31]\n"
    "s_waitcnt vmcnt(20)\n"
    "s_barrier\n"
    "v_mfma_f32_16x16x32_bf16 v[32:35], a[192:195], a[160:163], 0\n"
    "v_mfma_f32_16x16x32_bf16 v[40:43], a[192:195], a[176:179], 0\n"
    "v_mfma_f32_16x16x32_bf16 v[36:39], a[208:211], a[160:163], 0\n"
    "ds_read_b128 a[192:195], v112 offset:16384\n"
    "v_mfma_f32_16x16x32_bf16 v[44:47], a[208:211], a[176:179], 0\n"
    "v_max3_f32 v2, v16, v17, v18\n"
    "v_max3_f32 v3, v24, v25, v26\n"
    "v_max3_f32 v2, v2, v19, v20\n"
    "v_max3_f32 v3, v3, v27, v28\n"
    "v_max3_f32 v2, v2, v21, v22\n"
    "v_mfma_f32_16x16x32_bf16 v[32:35], a[196:199], a[164:167], v[32:35]\n"
    "ds_read_b128 a[208:211], v112 offset:20480\n"
    "v_max3_f32 v3, v3, v29, v30\n"
    "v_max_f32 v2, v2, v23\n"
    "v_max_f32 v3, v3, v31\n"
    "v_mov_b32 v4, v2\n"
    "v_mov_b32 v5, v3\n"
    "v_mfma_f32_16x16x32_bf16 v[40:43], a[196:199], a[180:183], v[40:43]\n"
    "s_nop 0\n"
    "v_permlane16_swap_b32 v4, v2\n"
    "v_permlane16_swap_b32 v5, v3\n"
    "v_max_f32 v2, v2, v4\n"
    "v_max_f32 v3, v3, v5\n"
    "v_mfma_f32_16x16x32_bf16 v[36:39], a[212:215], a[164:167], v[36:39]\n"
    "ds_read_b128 a[196:199], v113 offset:16384\n"
    "v_mov_b32 v4, v2\n"
    "v_mov_b32 v5, v3\n"
    "s_nop 0\n"
    "v_permlane32_swap_b32 v4, v2\n"
    "v_permlane32_swap_b32 v5, v3\n"
    "v_mfma_f32_16x16x32_bf16 v[44:47], a[212:215], a[180:183], v[44:47]\n"
    "v_max_f32 v2, v2, v4\n"
    "v_max_f32 v3, v3, v5\n"
    "v_mov_b32 v138, v2\n"
    "v_mov_b32 v139, v3\n"
    "v_sub_f32 v80, 0, v138\n"
    "v_mfma_f32_16x16x32_bf16 v[32:35], a[200:203], a[168:171], v[32:35]\n"
    "ds_read_b128 a[212:215], v113 offset:20480\n"
    "v_sub_f32 v81, 0, v138\n"
    "v_sub_f32 v82, 0, v138\n"
    "v_sub_f32 v83, 0, v138\n"
    "v_sub_f32 v84, 0, v139\n"
    "v_sub_f32 v85, 0, v139\n"
    "v_mfma_f32_16x16x32_bf16 v[40:43], a[200:203], a[184:187], v[40:43]\n"
    "v_sub_f32 v86, 0, v139\n"
    "v_sub_f32 v87, 0, v139\n"
    "v_sub_f32 v16, v16, v2\n"
    "v_sub_f32 v17, v17, v2\n"
    "v_sub_f32 v18, v18, v2\n"
    "v_mfma_f32_16x16x32_bf16 v[36:39], a[216:219], a[168:171], v[36:39]\n"
    "ds_read_b128 a[200:203], v114 offset:16384\n"
    "v_sub_f32 v19, v19, v2\n"
    "v_sub_f32 v20, v20, v2\n"
    "v_sub_f32 v21, v21, v2\n"
    "v_sub_f32 v22, v22, v2\n"
    "v_sub_f32 v23, v23, v2\n"
    "v_mfma_f32_16x16x32_bf16 v[44:47], a[216:219], a[184:187], v[44:47]\n"
    "v_sub_f32 v24, v24, v3\n"
    "v_sub_f32 v25, v25, v3\n"
    "v_sub_f32 v26, v26, v3\n"
    "v_sub_f32 v27, v27, v3\n"
    "v_sub_f32 v28, v28, v3\n"
    "v_mfma_f32_16x16x32_bf16 v[32:35], a[204:207], a[172:175], v[32:35]\n"
    "ds_read_b128 a[216:219], v114 offset:20480\n"
    "v_sub_f32 v29, v29, v3\n"
    "v_sub_f32 v30, v30, v3\n"
    "v_sub_f32 v31, v31, v3\n"
    "v_exp_f32 v48, v16\n"
    "v_mfma_f32_16x16x32_bf16 v[40:43], a[204:207], a[188:191], v[40:43]\n"
    "v_exp_f32 v49, v17\n"
    "v_exp_f32 v50, v18\n"
    "v_exp_f32 v51, v19\n"
    "v_mfma_f32_16x16x32_bf16 v[36:39], a[220:223], a[172:175], v[36:39]\n"
    "ds_read_b128 a[204:207], v115 offset:16384\n"
    "v_exp_f32 v52, v20\n"
    "v_exp_f32 v53, v21\n"
    "v_mfma_f32_16x16x32_bf16 v[44:47], a[220:223], a[188:191], v[44:47]\n"
    "ds_read_b128 a[220:223], v115 offset:20480\n"
    "v_exp_f32 v54, v22\n"
    "v_exp_f32 v55, v23\n"
    "s_waitcnt vmcnt(12) lgkmcnt(0)\n"
    "s_barrier\n"
    "s_add_u32 m0, s71, 57344\n"
    "s_nop 0\n"
    "buffer_load_dwordx4 v128, s[44:47], s67 offen offset:0 nt lds\n"
    "buffer_load_dwordx4 v129, s[44:47], s67 offen offset:1024 nt lds\n"
    "buffer_load_dwordx4 v128, s[44:47], s67 offen offset:2048 nt lds\n"
    "buffer_load_dwordx4 v129, s[44:47], s67 offen offset:3072 nt lds\n"
    "s_add_u32 s67, s67, 16384\n"
    "ds_read_b64_tr_b16 a[240:241], v120 offset:0\n"
    "ds_read_b64_tr_b16 a[242:243], v120 offset:4096\n"
    "ds_read_b64_tr_b16 a[244:245], v121 offset:0\n"
    "ds_read_b64_tr_b16 a[246:247], v121 offset:4096\n"
    "ds_read_b64_tr_b16 a[248:249], v122 offset:0\n"
    "ds_read_b64_tr_b16 a[250:251], v122 offset:4096\n"
    "ds_read_b64_tr_b16 a[252:253], v123 offset:0\n"
    "ds_read_b64_tr_b16 a[254:255], v123 offset:4096\n"
    "ds_read_b64_tr_b16 a[224:225], v116 offset:0\n"
    "ds_read_b64_tr_b16 a[226:227], v116 offset:4096\n"
    "ds_read_b64_tr_b16 a[228:229], v117 offset:0\n"
    "ds_read_b64_tr_b16 a[230:231], v117 offset:4096\n"
    "ds_read_b64_tr_b16 a[232:233], v118 offset:0\n"
    "ds_read_b64_tr_b16 a[234:235], v118 offset:4096\n"
    "ds_read_b64_tr_b16 a[236:237], v119 offset:0\n"
    "ds_read_b64_tr_b16 a[238:239], v119 offset:4096\n"
    "v_exp_f32 v56, v24\n"
    "v_exp_f32 v57, v25\n"
    "v_exp_f32 v58, v26\n"
    "v_exp_f32 v59, v27\n"
    "v_exp_f32 v60, v28\n"
    "v_exp_f32 v61, v29\n"
    "v_exp_f32 v62, v30\n"
    "v_exp_f32 v63, v31\n"
    "v_mfma_f32_16x16x32_bf16 v[16:19], a[192:195], a[128:131], v[80:83]\n"
    "v_add_f32 v130, v48, v49\n"
    "v_max3_f32 v2, v32, v33, v34\n"
    "v_max3_f32 v3, v40, v41, v42\n"
    "v_max3_f32 v2, v2, v35, v36\n"
    "v_add_f32 v131, v56, v57\n"
    "v_mfma_f32_16x16x32_bf16 v[24:27], a[192:195], a[144:147], v[84:87]\n"
    "v_max3_f32 v3, v3, v43, v44\n"
    "v_max3_f32 v2, v2, v37, v38\n"
    "v_add_f32 v130, v130, v50\n"
    "v_max3_f32 v3, v3, v45, v46\n"
    "v_max_f32 v2, v2, v39\n"
    "v_mfma_f32_16x16x32_bf16 v[20:23], a[208:211], a[128:131], v[80:83]\n"
    "v_add_f32 v131, v131, v58\n"
    "v_max_f32 v3, v3, v47\n"
    "v_mov_b32 v4, v2\n"
    "v_mov_b32 v5, v3\n"
    "v_add_f32 v130, v130, v51\n"
    "v_mfma_f32_16x16x32_bf16 v[28:31], a[208:211], a[144:147], v[84:87]\n"
    "s_nop 0\n"
    "v_permlane16_swap_b32 v4, v2\n"
    "v_add_f32 v131, v131, v59\n"
    "v_permlane16_swap_b32 v5, v3\n"
    "v_max_f32 v2, v2, v4\n"
    "v_mfma_f32_16x16x32_bf16 v[16:19], a[196:199], a[132:135], v[16:19]\n"
    "v_add_f32 v130, v130, v52\n"
    "v_max_f32 v3, v3, v5\n"
    "v_mov_b32 v4, v2\n"
    "v_mov_b32 v5, v3\n"
    "v_add_f32 v131, v131, v60\n"
    "v_mfma_f32_16x16x32_bf16 v[24:27], a[196:199], a[148:151], v[24:27]\n"
    "s_nop 0\n"
    "v_permlane32_swap_b32 v4, v2\n"
    "v_add_f32 v130, v130, v53\n"
    "v_permlane32_swap_b32 v5, v3\n"
    "v_max_f32 v2, v2, v4\n"
    "v_mfma_f32_16x16x32_bf16 v[20:23], a[212:215], a[132:135], v[20:23]\n"
    "v_add_f32 v131, v131, v61\n"
    "v_max_f32 v3, v3, v5\n"
    "v_mov_b32 v140, v2\n"
    "v_mov_b32 v141, v3\n"
    "v_add_f32 v130, v130, v54\n"
    "v_mfma_f32_16x16x32_bf16 v[28:31], a[212:215], a[148:151], v[28:31]\n"
    "v_sub_f32 v88, 0, v140\n"
    "v_sub_f32 v89, 0, v140\n"
    "v_add_f32 v131, v131, v62\n"
    "v_sub_f32 v90, 0, v140\n"
    "v_sub_f32 v91, 0, v140\n"
    "v_mfma_f32_16x16x32_bf16 v[16:19], a[200:203], a[136:139], v[16:19]\n"
    "v_add_f32 v130, v130, v55\n"
    "v_sub_f32 v92, 0, v141\n"
    "v_sub_f32 v93, 0, v141\n"
    "v_sub_f32 v94, 0, v141\n"
    "v_add_f32 v131, v131, v63\n"
    "v_mfma_f32_16x16x32_bf16 v[24:27], a[200:203], a[152:155], v[24:27]\n"
    "v_sub_f32 v95, 0, v141\n"
    "v_sub_f32 v32, v32, v2\n"
    "v_cvt_pk_bf16_f32 v96, v48, v49\n"
    "v_sub_f32 v33, v33, v2\n"
    "v_sub_f32 v34, v34, v2\n"
    "v_mfma_f32_16x16x32_bf16 v[20:23], a[216:219], a[136:139], v[20:23]\n"
    "v_cvt_pk_bf16_f32 v97, v50, v51\n"
    "v_sub_f32 v35, v35, v2\n"
    "v_sub_f32 v36, v36, v2\n"
    "v_sub_f32 v37, v37, v2\n"
    "v_cvt_pk_bf16_f32 v98, v52, v53\n"
    "v_mfma_f32_16x16x32_bf16 v[28:31], a[216:219], a[152:155], v[28:31]\n"
    "v_sub_f32 v38, v38, v2\n"
    "v_sub_f32 v39, v39, v2\n"
    "v_cvt_pk_bf16_f32 v99, v54, v55\n"
    "v_sub_f32 v40, v40, v3\n"
    "v_sub_f32 v41, v41, v3\n"
    "v_mfma_f32_16x16x32_bf16 v[16:19], a[204:207], a[140:143], v[16:19]\n"
    "v_cvt_pk_bf16_f32 v100, v56, v57\n"
    "v_sub_f32 v42, v42, v3\n"
    "v_sub_f32 v43, v43, v3\n"
    "v_sub_f32 v44, v44, v3\n"
    "v_cvt_pk_bf16_f32 v101, v58, v59\n"
    "v_mfma_f32_16x16x32_bf16 v[24:27], a[204:207], a[156:159], v[24:27]\n"
    "v_sub_f32 v45, v45, v3\n"
    "v_sub_f32 v46, v46, v3\n"
    "v_cvt_pk_bf16_f32 v102, v60, v61\n"
    "v_sub_f32 v47, v47, v3\n"
    "v_exp_f32 v64, v32\n"
    "v_mfma_f32_16x16x32_bf16 v[20:23], a[220:223], a[140:143], v[20:23]\n"
    "v_cvt_pk_bf16_f32 v103, v62, v63\n"
    "v_exp_f32 v65, v33\n"
    "v_mov_b32 v134, v130\n"
    "v_mfma_f32_16x16x32_bf16 v[28:31], a[220:223], a[156:159], v[28:31]\n"
    "v_exp_f32 v66, v34\n"
    "v_mov_b32 v135, v131\n"
    "s_waitcnt lgkmcnt(0)\n"
    "v_exp_f32 v67, v35\n"
    "s_add_u32 m0, s71, 16384\n"
    "s_nop 0\n"
    "buffer_load_dwordx4 v124, s[40:43], s66 offen offset:0 nt lds\n"
    "buffer_load_dwordx4 v125, s[40:43], s66 offen offset:1024 nt lds\n"
    "buffer_load_dwordx4 v126, s[40:43], s66 offen offset:2048 nt lds\n"
    "buffer_load_dwordx4 v127, s[40:43], s66 offen offset:3072 nt lds\n"
    "s_add_u32 s66, s66, 16384\n"
    "v_mfma_f32_16x16x32_bf16 a[0:3], a[240:243], v[96:99], 0\n"
    "v_exp_f32 v68, v36\n"
    "v_exp_f32 v69, v37\n"
    "v_mfma_f32_16x16x32_bf16 a[16:19], a[240:243], v[100:103], 0\n"
    "v_exp_f32 v70, v38\n"
    "v_exp_f32 v71, v39\n"
    "v_mfma_f32_16x16x32_bf16 a[4:7], a[244:247], v[96:99], 0\n"
    "v_exp_f32 v72, v40\n"
    "v_exp_f32 v73, v41\n"
    "v_mfma_f32_16x16x32_bf16 a[20:23], a[244:247], v[100:103], 0\n"
    "v_exp_f32 v48, v16\n"
    "v_exp_f32 v74, v42\n"
    "v_mfma_f32_16x16x32_bf16 a[8:11], a[248:251], v[96:99], 0\n"
    "v_exp_f32 v49, v17\n"
    "v_exp_f32 v75, v43\n"
    "v_mfma_f32_16x16x32_bf16 a[24:27], a[248:251], v[100:103], 0\n"
    "v_exp_f32 v50, v18\n"
    "v_exp_f32 v76, v44\n"
    "v_mfma_f32_16x16x32_bf16 a[12:15], a[252:255], v[96:99], 0\n"
    "v_exp_f32 v77, v45\n"
    "v_exp_f32 v51, v19\n"
    "v_mfma_f32_16x16x32_bf16 a[28:31], a[252:255], v[100:103], 0\n"
    "v_exp_f32 v78, v46\n"
    "v_exp_f32 v52, v20\n"
    "v_mfma_f32_16x16x32_bf16 v[192:195], a[224:227], v[96:99], 0\n"
    "v_exp_f32 v79, v47\n"
    "v_add_f32 v132, v64, v65\n"
    "v_exp_f32 v53, v21\n"
    "v_mfma_f32_16x16x32_bf16 v[208:211], a[224:227], v[100:103], 0\n"
    "v_add_f32 v133, v72, v73\n"
    "v_add_f32 v132, v132, v66\n"
    "v_add_f32 v133, v133, v74\n"
    "v_add_f32 v132, v132, v67\n"
    "v_exp_f32 v54, v22\n"
    "v_mfma_f32_16x16x32_bf16 v[196:199], a[228:231], v[96:99], 0\n"
    "v_add_f32 v133, v133, v75\n"
    "v_add_f32 v132, v132, v68\n"
    "v_add_f32 v133, v133, v76\n"
    "v_exp_f32 v55, v23\n"
    "v_mfma_f32_16x16x32_bf16 v[212:215], a[228:231], v[100:103], 0\n"
    "v_add_f32 v132, v132, v69\n"
    "v_add_f32 v133, v133, v77\n"
    "v_add_f32 v132, v132, v70\n"
    "v_exp_f32 v56, v24\n"
    "v_mfma_f32_16x16x32_bf16 v[200:203], a[232:235], v[96:99], 0\n"
    "v_add_f32 v133, v133, v78\n"
    "v_add_f32 v132, v132, v71\n"
    "v_add_f32 v133, v133, v79\n"
    "v_cvt_pk_bf16_f32 v104, v64, v65\n"
    "v_mfma_f32_16x16x32_bf16 v[216:219], a[232:235], v[100:103], 0\n"
    "v_exp_f32 v57, v25\n"
    "v_cvt_pk_bf16_f32 v105, v66, v67\n"
    "v_cvt_pk_bf16_f32 v106, v68, v69\n"
    "v_cvt_pk_bf16_f32 v107, v70, v71\n"
    "v_mfma_f32_16x16x32_bf16 v[204:207], a[236:239], v[96:99], 0\n"
    "v_exp_f32 v58, v26\n"
    "v_cvt_pk_bf16_f32 v108, v72, v73\n"
    "v_cvt_pk_bf16_f32 v109, v74, v75\n"
    "v_cvt_pk_bf16_f32 v110, v76, v77\n"
    "v_mfma_f32_16x16x32_bf16 v[220:223], a[236:239], v[100:103], 0\n"
    "v_exp_f32 v59, v27\n"
    "v_cvt_pk_bf16_f32 v111, v78, v79\n"
    "v_mov_b32 v136, v132\n"
    "v_mov_b32 v137, v133\n"
    "v_mfma_f32_16x16x32_bf16 v[32:35], a[192:195], a[160:163], v[88:91]\n"
    "v_exp_f32 v60, v28\n"
    "v_mfma_f32_16x16x32_bf16 v[40:43], a[192:195], a[176:179], v[92:95]\n"
    "v_cvt_pk_bf16_f32 v96, v48, v49\n"
    "v_exp_f32 v61, v29\n"
    "v_mfma_f32_16x16x32_bf16 v[36:39], a[208:211], a[160:163], v[88:91]\n"
    "v_exp_f32 v62, v30\n"
    "v_mfma_f32_16x16x32_bf16 v[44:47], a[208:211], a[176:179], v[92:95]\n"
    "v_cvt_pk_bf16_f32 v97, v50, v51\n"
    "v_exp_f32 v63, v31\n"
    "v_mfma_f32_16x16x32_bf16 v[32:35], a[196:199], a[164:167], v[32:35]\n"
    "v_add_f32 v130, v48, v49\n"
    "v_add_f32 v131, v56, v57\n"
    "v_mfma_f32_16x16x32_bf16 v[40:43], a[196:199], a[180:183], v[40:43]\n"
    "v_add_f32 v130, v130, v50\n"
    "v_add_f32 v131, v131, v58\n"
    "v_mfma_f32_16x16x32_bf16 v[36:39], a[212:215], a[164:167], v[36:39]\n"
    "v_cvt_pk_bf16_f32 v98, v52, v53\n"
    "v_add_f32 v130, v130, v51\n"
    "v_mfma_f32_16x16x32_bf16 v[44:47], a[212:215], a[180:183], v[44:47]\n"
    "v_add_f32 v131, v131, v59\n"
    "v_add_f32 v130, v130, v52\n"
    "v_mfma_f32_16x16x32_bf16 v[32:35], a[200:203], a[168:171], v[32:35]\n"
    "v_add_f32 v131, v131, v60\n"
    "v_add_f32 v130, v130, v53\n"
    "v_mfma_f32_16x16x32_bf16 v[40:43], a[200:203], a[184:187], v[40:43]\n"
    "v_cvt_pk_bf16_f32 v99, v54, v55\n"
    "v_add_f32 v131, v131, v61\n"
    "v_mfma_f32_16x16x32_bf16 v[36:39], a[216:219], a[168:171], v[36:39]\n"
    "v_add_f32 v130, v130, v54\n"
    "v_add_f32 v131, v131, v62\n"
    "v_mfma_f32_16x16x32_bf16 v[44:47], a[216:219], a[184:187], v[44:47]\n"
    "v_add_f32 v130, v130, v55\n"
    "v_add_f32 v131, v131, v63\n"
    "v_mfma_f32_16x16x32_bf16 v[32:35], a[204:207], a[172:175], v[32:35]\n"
    "v_mfma_f32_16x16x32_bf16 v[40:43], a[204:207], a[188:191], v[40:43]\n"
    "v_mfma_f32_16x16x32_bf16 v[36:39], a[220:223], a[172:175], v[36:39]\n"
    "v_mfma_f32_16x16x32_bf16 v[44:47], a[220:223], a[188:191], v[44:47]\n"
    "v_mfma_f32_16x16x32_bf16 a[32:35], a[240:243], v[104:107], 0\n"
    "ds_read_b128 a[192:195], v112 offset:32768\n"
    "v_cvt_pk_bf16_f32 v100, v56, v57\n"
    "v_cvt_pk_bf16_f32 v101, v58, v59\n"
    "v_mfma_f32_16x16x32_bf16 a[48:51], a[240:243], v[108:111], 0\n"
    "ds_read_b128 a[208:211], v112 offset:36864\n"
    "v_cvt_pk_bf16_f32 v102, v60, v61\n"
    "v_cvt_pk_bf16_f32 v103, v62, v63\n"
    "v_mfma_f32_16x16x32_bf16 a[36:39], a[244:247], v[104:107], 0\n"
    "ds_read_b128 a[196:199], v113 offset:32768\n"
    "v_max_f32 v15, v130, v131\n"
    "v_cmp_nge_f32 vcc, s76, v15\n"
    "v_mfma_f32_16x16x32_bf16 a[52:55], a[244:247], v[108:111], 0\n"
    "ds_read_b128 a[212:215], v113 offset:36864\n"
    "v_exp_f32 v64, v32\n"
    "v_mfma_f32_16x16x32_bf16 a[40:43], a[248:251], v[104:107], 0\n"
    "ds_read_b128 a[200:203], v114 offset:32768\n"
    "v_exp_f32 v65, v33\n"
    "v_mfma_f32_16x16x32_bf16 a[56:59], a[248:251], v[108:111], 0\n"
    "ds_read_b128 a[216:219], v114 offset:36864\n"
    "v_exp_f32 v66, v34\n"
    "v_mfma_f32_16x16x32_bf16 a[44:47], a[252:255], v[104:107], 0\n"
    "ds_read_b128 a[204:207], v115 offset:32768\n"
    "v_exp_f32 v67, v35\n"
    "v_mfma_f32_16x16x32_bf16 a[60:63], a[252:255], v[108:111], 0\n"
    "ds_read_b128 a[220:223], v115 offset:36864\n"
    "v_exp_f32 v68, v36\n"
    "v_mfma_f32_16x16x32_bf16 v[224:227], a[224:227], v[104:107], 0\n"
    "v_mfma_f32_16x16x32_bf16 v[240:243], a[224:227], v[108:111], 0\n"
    "v_mfma_f32_16x16x32_bf16 v[228:231], a[228:231], v[104:107], 0\n"
    "v_exp_f32 v69, v37\n"
    "v_mfma_f32_16x16x32_bf16 v[244:247], a[228:231], v[108:111], 0\n"
    "v_mfma_f32_16x16x32_bf16 v[232:235], a[232:235], v[104:107], 0\n"
    "v_exp_f32 v70, v38\n"
    "v_exp_f32 v71, v39\n"
    "v_mfma_f32_16x16x32_bf16 v[248:251], a[232:235], v[108:111], 0\n"
    "s_memrealtime s[34:35]\n"
    "v_exp_f32 v72, v40\n"
    "v_mfma_f32_16x16x32_bf16 v[236:239], a[236:239], v[104:107], 0\n"
    "v_exp_f32 v73, v41\n"
    "v_exp_f32 v74, v42\n"
    "v_mfma_f32_16x16x32_bf16 v[252:255], a[236:239], v[108:111], 0\n"
    "v_exp_f32 v75, v43\n"
    "s_waitcnt lgkmcnt(0)\n"
    "s_mov_b32 s80, s34\n"
    "s_branch .Lattn_1\n"
    ".Lattn_loop:\n"
    "s_cbranch_vccnz .Lrare_a0\n"
    ".Lback_a0:\n"
    "s_waitcnt vmcnt(16) lgkmcnt(0)\n"
    "s_sub_u32 s28, s34, s80\n"
    "s_cmp_lt_i32 s28, 0\n"
    "s_cbranch_scc1 .Lwait_0\n"
    ".Lgo_0:\n"
    "s_add_u32 s80, s34, s77\n"
    "s_barrier\n"
    "v_mfma_f32_16x16x32_bf16 v[16:19], a[192:195], a[128:131], v[80:83]\n"
    "ds_read_b64_tr_b16 a[240:241], v120 offset:0\n"
    "v_exp_f32 v76, v44\n"
    "v_mfma_f32_16x16x32_bf16 v[24:27], a[192:195], a[144:147], v[84:87]\n"
    "ds_read_b64_tr_b16 a[242:243], v120 offset:4096\n"
    "v_add_f32 v132, v64, v65\n"
    "v_mfma_f32_16x16x32_bf16 v[20:23], a[208:211], a[128:131], v[80:83]\n"
    "ds_read_b64_tr_b16 a[244:245], v121 offset:0\n"
    "v_add_f32 v133, v72, v73\n"
    "v_mfma_f32_16x16x32_bf16 v[28:31], a[208:211], a[144:147], v[84:87]\n"
    "ds_read_b64_tr_b16 a[246:247], v121 offset:4096\n"
    "v_add_f32 v132, v132, v66\n"
    "v_mfma_f32_16x16x32_bf16 v[16:19], a[196:199], a[132:135], v[16:19]\n"
    "ds_read_b64_tr_b16 a[248:249], v122 offset:0\n"
    "v_exp_f32 v77, v45\n"
    "v_mfma_f32_16x16x32_bf16 v[24:27], a[196:199], a[148:151], v[24:27]\n"
    "ds_read_b64_tr_b16 a[250:251], v122 offset:4096\n"
    "v_add_f32 v133, v133, v74\n"
    "v_mfma_f32_16x16x32_bf16 v[20:23], a[212:215], a[132:135], v[20:23]\n"
    "ds_read_b64_tr_b16 a[252:253], v123 offset:0\n"
    "v_add_f32 v132, v132, v67\n"
    "v_mfma_f32_16x16x32_bf16 v[28:31], a[212:215], a[148:151], v[28:31]\n"
    "ds_read_b64_tr_b16 a[254:255], v123 offset:4096\n"
    "v_add_f32 v133, v133, v75\n"
    "v_mfma_f32_16x16x32_bf16 v[16:19], a[200:203], a[136:139], v[16:19]\n"
    "ds_read_b64_tr_b16 a[224:225], v116 offset:0\n"
    "v_exp_f32 v78, v46\n"
    "v_mfma_f32_16x16x32_bf16 v[24:27], a[200:203], a[152:155], v[24:27]\n"
    "ds_read_b64_tr_b16 a[226:227], v116 offset:4096\n"
    "v_add_f32 v132, v132, v68\n"
    "v_mfma_f32_16x16x32_bf16 v[20:23], a[216:219], a[136:139], v[20:23]\n"
    "ds_read_b64_tr_b16 a[228:229], v117 offset:0\n"
    "v_add_f32 v132, v132, v69\n"
    "v_mfma_f32_16x16x32_bf16 v[28:31], a[216:219], a[152:155], v[28:31]\n"
    "ds_read_b64_tr_b16 a[230:231], v117 offset:4096\n"
    "v_cvt_pk_bf16_f32 v104, v64, v65\n"
    "v_mfma_f32_16x16x32_bf16 v[16:19], a[204:207], a[140:143], v[16:19]\n"
    "ds_read_b64_tr_b16 a[232:233], v118 offset:0\n"
    "v_exp_f32 v79, v47\n"
    "v_mfma_f32_16x16x32_bf16 v[24:27], a[204:207], a[156:159], v[24:27]\n"
    "ds_read_b64_tr_b16 a[234:235], v118 offset:4096\n"
    "v_cvt_pk_bf16_f32 v105, v66, v67\n"
    "v_mfma_f32_16x16x32_bf16 v[20:23], a[220:223], a[140:143], v[20:23]\n"
    "ds_read_b64_tr_b16 a[236:237], v119 offset:0\n"
    "v_cvt_pk_bf16_f32 v106, v68, v69\n"
    "v_mfma_f32_16x16x32_bf16 v[28:31], a[220:223], a[156:159], v[28:31]\n"
    "ds_read_b64_tr_b16 a[238:239], v119 offset:4096\n"
    "s_waitcnt lgkmcnt(8)\n"
    "v_mfma_f32_16x16x32_bf16 a[0:3], a[240:243], v[96:99], a[0:3]\n"
    "s_add_u32 m0, s71, 57344\n"
    "v_add_f32 v134, v134, v130\n"
    "v_cvt_pk_bf16_f32 v107, v70, v71\n"
    "v_add_f32 v135, v135, v131\n"
    "v_mfma_f32_16x16x32_bf16 a[16:19], a[240:243], v[100:103], a[16:19]\n"
    "buffer_load_dwordx4 v128, s[44:47], s67 offen offset:0 nt lds\n"
    "v_add_f32 v133, v133, v76\n"
    "v_cvt_pk_bf16_f32 v108, v72, v73\n"
    "v_mfma_f32_16x16x32_bf16 a[4:7], a[244:247], v[96:99], a[4:7]\n"
    "buffer_load_dwordx4 v129, s[44:47], s67 offen offset:1024 nt lds\n"
    "v_add_f32 v133, v133, v77\n"
    "v_add_f32 v132, v132, v70\n"
    "v_mfma_f32_16x16x32_bf16 a[20:23], a[244:247], v[100:103], a[20:23]\n"
    "v_exp_f32 v48, v16\n"
    "v_mfma_f32_16x16x32_bf16 a[8:11], a[248:251], v[96:99], a[8:11]\n"
    "v_exp_f32 v49, v17\n"
    "v_mfma_f32_16x16x32_bf16 a[24:27], a[248:251], v[100:103], a[24:27]\n"
    "v_exp_f32 v50, v18\n"
    "v_cvt_pk_bf16_f32 v109, v74, v75\n"
    "v_mfma_f32_16x16x32_bf16 a[12:15], a[252:255], v[96:99], a[12:15]\n"
    "v_exp_f32 v51, v19\n"
    "v_add_f32 v133, v133, v78\n"
    "v_mfma_f32_16x16x32_bf16 a[28:31], a[252:255], v[100:103], a[28:31]\n"
    "s_waitcnt lgkmcnt(0)\n"
    "v_exp_f32 v52, v20\n"
    "v_mfma_f32_16x16x32_bf16 v[192:195], a[224:227], v[96:99], v[192:195]\n"
    "buffer_load_dwordx4 v128, s[44:47], s67 offen offset:2048 nt lds\n"
    "v_exp_f32 v53, v21\n"
    "v_mfma_f32_16x16x32_bf16 v[208:211], a[224:227], v[100:103], v[208:211]\n"
    "v_add_f32 v132, v132, v71\n"
    "v_cvt_pk_bf16_f32 v110, v76, v77\n"
    "v_mfma_f32_16x16x32_bf16 v[196:199], a[228:231], v[96:99], v[196:199]\n"
    "v_exp_f32 v54, v22\n"
    "v_add_f32 v133, v133, v79\n"
    "v_mfma_f32_16x16x32_bf16 v[212:215], a[228:231], v[100:103], v[212:215]\n"
    "v_exp_f32 v55, v23\n"
    "v_mfma_f32_16x16x32_bf16 v[200:203], a[232:235], v[96:99], v[200:203]\n"
    "buffer_load_dwordx4 v129, s[44:47], s67 offen offset:3072 nt lds\n"
    "v_max_f32 v15, v132, v133\n"
    "s_add_u32 s67, s67, 16384\n"
    "v_mfma_f32_16x16x32_bf16 v[216:219], a[232:235], v[100:103], v[216:219]\n"
    "v_exp_f32 v56, v24\n"
    "v_cvt_pk_bf16_f32 v111, v78, v79\n"
    "v_mfma_f32_16x16x32_bf16 v[204:207], a[236:239], v[96:99], v[204:207]\n"
    "v_cmp_nge_f32 vcc, s76, v15\n"
    "v_exp_f32 v57, v25\n"
    "v_mfma_f32_16x16x32_bf16 v[220:223], a[236:239], v[100:103], v[220:223]\n"
    "v_exp_f32 v58, v26\n"
    "s_cbranch_vccnz .Lrare_b0\n"
    ".Lback_b0:\n"
    "v_mfma_f32_16x16x32_bf16 v[32:35], a[192:195], a[160:163], v[88:91]\n"
    "s_add_u32 m0, s71, 16384\n"
    "v_exp_f32 v59, v27\n"
    "v_mfma_f32_16x16x32_bf16 v[40:43], a[192:195], a[176:179], v[92:95]\n"
    "buffer_load_dwordx4 v124, s[40:43], s66 offen offset:0 nt lds\n"
    "v_add_f32 v136, v136, v132\n"
    "v_mfma_f32_16x16x32_bf16 v[36:39], a[208:211], a[160:163], v[88:91]\n"
    "v_cvt_pk_bf16_f32 v96, v48, v49\n"
    "v_add_f32 v137, v137, v133\n"
    "v_mfma_f32_16x16x32_bf16 v[44:47], a[208:211], a[176:179], v[92:95]\n"
    "v_exp_f32 v60, v28\n"
    "v_mfma_f32_16x16x32_bf16 v[32:35], a[196:199], a[164:167], v[32:35]\n"
    "v_add_f32 v130, v48, v49\n"
    "v_cvt_pk_bf16_f32 v97, v50, v51\n"
    "v_mfma_f32_16x16x32_bf16 v[40:43], a[196:199], a[180:183], v[40:43]\n"
    "v_exp_f32 v61, v29\n"
    "v_mfma_f32_16x16x32_bf16 v[36:39], a[212:215], a[164:167], v[36:39]\n"
    "buffer_load_dwordx4 v125, s[40:43], s66 offen offset:1024 nt lds\n"
    "v_add_f32 v131, v56, v57\n"
    "v_mfma_f32_16x16x32_bf16 v[44:47], a[212:215], a[180:183], v[44:47]\n"
    "v_add_f32 v130, v130, v50\n"
    "v_exp_f32 v62, v30\n"
    "v_mfma_f32_16x16x32_bf16 v[32:35], a[200:203], a[168:171], v[32:35]\n"
    "v_cvt_pk_bf16_f32 v98, v52, v53\n"
    "v_add_f32 v131, v131, v58\n"
    "v_mfma_f32_16x16x32_bf16 v[40:43], a[200:203], a[184:187], v[40:43]\n"
    "v_exp_f32 v63, v31\n"
    "v_mfma_f32_16x16x32_bf16 v[36:39], a[216:219], a[168:171], v[36:39]\n"
    "buffer_load_dwordx4 v126, s[40:43], s66 offen offset:2048 nt lds\n"
    "v_add_f32 v130, v130, v51\n"
    "v_mfma_f32_16x16x32_bf16 v[44:47], a[216:219], a[184:187], v[44:47]\n"
    "v_add_f32 v131, v131, v59\n"
    "v_cvt_pk_bf16_f32 v99, v54, v55\n"
    "v_mfma_f32_16x16x32_bf16 v[32:35], a[204:207], a[172:175], v[32:35]\n"
    "v_add_f32 v130, v130, v52\n"
    "v_add_f32 v131, v131, v60\n"
    "v_mfma_f32_16x16x32_bf16 v[40:43], a[204:207], a[188:191], v[40:43]\n"
    "v_cvt_pk_bf16_f32 v100, v56, v57\n"
    "v_add_f32 v130, v130, v53\n"
    "v_mfma_f32_16x16x32_bf16 v[36:39], a[220:223], a[172:175], v[36:39]\n"
    "buffer_load_dwordx4 v127, s[40:43], s66 offen offset:3072 nt lds\n"
    "s_add_u32 s66, s66, 16384\n"
    "v_add_f32 v131, v131, v61\n"
    "v_cvt_pk_bf16_f32 v101, v58, v59\n"
    "v_mfma_f32_16x16x32_bf16 v[44:47], a[220:223], a[188:191], v[44:47]\n"
    "v_add_f32 v130, v130, v54\n"
    "v_add_f32 v131, v131, v62\n"
    "v_mfma_f32_16x16x32_bf16 a[32:35], a[240:243], v[104:107], a[32:35]\n"
    "ds_read_b128 a[192:195], v112 offset:32768\n"
    "v_add_f32 v130, v130, v55\n"
    "v_mfma_f32_16x16x32_bf16 a[48:51], a[240:243], v[108:111], a[48:51]\n"
    "ds_read_b128 a[208:211], v112 offset:36864\n"
    "v_cvt_pk_bf16_f32 v102, v60, v61\n"
    "v_mfma_f32_16x16x32_bf16 a[36:39], a[244:247], v[104:107], a[36:39]\n"
    "ds_read_b128 a[196:199], v113 offset:32768\n"
    "v_add_f32 v131, v131, v63\n"
    "v_mfma_f32_16x16x32_bf16 a[52:55], a[244:247], v[108:111], a[52:55]\n"
    "v_exp_f32 v64, v32\n"
    "v_mfma_f32_16x16x32_bf16 a[40:43], a[248:251], v[104:107], a[40:43]\n"
    "ds_read_b128 a[212:215], v113 offset:36864\n"
    "v_exp_f32 v65, v33\n"
    "v_mfma_f32_16x16x32_bf16 a[56:59], a[248:251], v[108:111], a[56:59]\n"
    "ds_read_b128 a[200:203], v114 offset:32768\n"
    "v_exp_f32 v66, v34\n"
    "v_mfma_f32_16x16x32_bf16 a[44:47], a[252:255], v[104:107], a[44:47]\n"
    "ds_read_b128 a[216:219], v114 offset:36864\n"
    "v_exp_f32 v67, v35\n"
    "v_mfma_f32_16x16x32_bf16 a[60:63], a[252:255], v[108:111], a[60:63]\n"
    "v_exp_f32 v68, v36\n"
    "v_mfma_f32_16x16x32_bf16 v[224:227], a[224:227], v[104:107], v[224:227]\n"
    "ds_read_b128 a[204:207], v115 offset:32768\n"
    "v_max_f32 v15, v130, v131\n"
    "v_mfma_f32_16x16x32_bf16 v[240:243], a[224:227], v[108:111], v[240:243]\n"
    "ds_read_b128 a[220:223], v115 offset:36864\n"
    "v_cvt_pk_bf16_f32 v103, v62, v63\n"
    "v_mfma_f32_16x16x32_bf16 v[228:231], a[228:231], v[104:107], v[228:231]\n"
    "v_exp_f32 v69, v37\n"
    "v_mfma_f32_16x16x32_bf16 v[244:247], a[228:231], v[108:111], v[244:247]\n"
    "v_exp_f32 v70, v38\n"
    "v_mfma_f32_16x16x32_bf16 v[232:235], a[232:235], v[104:107], v[232:235]\n"
    "v_exp_f32 v71, v39\n"
    "v_cmp_nge_f32 vcc, s76, v15\n"
    "v_mfma_f32_16x16x32_bf16 v[248:251], a[232:235], v[108:111], v[248:251]\n"
    "s_memrealtime s[34:35]\n"
    "v_exp_f32 v72, v40\n"
    "v_mfma_f32_16x16x32_bf16 v[236:239], a[236:239], v[104:107], v[236:239]\n"
    "v_exp_f32 v73, v41\n"
    "v_exp_f32 v74, v42\n"
    "v_mfma_f32_16x16x32_bf16 v[252:255], a[236:239], v[108:111], v[252:255]\n"
    "v_exp_f32 v75, v43\n"
    ".Lattn_1:\n"
    "s_cbranch_vccnz .Lrare_a1\n"
    ".Lback_a1:\n"
    "s_waitcnt vmcnt(16) lgkmcnt(0)\n"
    "s_sub_u32 s28, s34, s80\n"
    "s_cmp_lt_i32 s28, 0\n"
    "s_cbranch_scc1 .Lwait_1\n"
    ".Lgo_1:\n"
    "s_add_u32 s80, s34, s77\n"
    "s_barrier\n"
    "v_mfma_f32_16x16x32_bf16 v[16:19], a[192:195], a[128:131], v[80:83]\n"
    "ds_read_b64_tr_b16 a[240:241], v120 offset:16384\n"
    "v_exp_f32 v76, v44\n"
    "v_mfma_f32_16x16x32_bf16 v[24:27], a[192:195], a[144:147], v[84:87]\n"
    "ds_read_b64_tr_b16 a[242:243], v120 offset:20480\n"
    "v_add_f32 v132, v64, v65\n"
    "v_mfma_f32_16x16x32_bf16 v[20:23], a[208:211], a[128:131], v[80:83]\n"
    "ds_read_b64_tr_b16 a[244:245], v121 offset:16384\n"
    "v_add_f32 v133, v72, v73\n"
    "v_mfma_f32_16x16x32_bf16 v[28:31], a[208:211], a[144:147], v[84:87]\n"
    "ds_read_b64_tr_b16 a[246:247], v121 offset:20480\n"
    "v_add_f32 v132, v132, v66\n"
    "v_mfma_f32_16x16x32_bf16 v[16:19], a[196:199], a[132:135], v[16:19]\n"
    "ds_read_b64_tr_b16 a[248:249], v122 offset:16384\n"
    "v_exp_f32 v77, v45\n"
    "v_mfma_f32_16x16x32_bf16 v[24:27], a[196:199], a[148:151], v[24:27]\n"
    "ds_read_b64_tr_b16 a[250:251], v122 offset:20480\n"
    "v_add_f32 v133, v133, v74\n"
    "v_mfma_f32_16x16x32_bf16 v[20:23], a[212:215], a[132:135], v[20:23]\n"
    "ds_read_b64_tr_b16 a[252:253], v123 offset:16384\n"
    "v_add_f32 v132, v132, v67\n"
    "v_mfma_f32_16x16x32_bf16 v[28:31], a[212:215], a[148:151], v[28:31]\n"
    "ds_read_b64_tr_b16 a[254:255], v123 offset:20480\n"
    "v_add_f32 v133, v133, v75\n"
    "v_mfma_f32_16x16x32_bf16 v[16:19], a[200:203], a[136:139], v[16:19]\n"
    "ds_read_b64_tr_b16 a[224:225], v116 offset:16384\n"
    "v_exp_f32 v78, v46\n"
    "v_mfma_f32_16x16x32_bf16 v[24:27], a[200:203], a[152:155], v[24:27]\n"
    "ds_read_b64_tr_b16 a[226:227], v116 offset:20480\n"
    "v_add_f32 v132, v132, v68\n"
    "v_mfma_f32_16x16x32_bf16 v[20:23], a[216:219], a[136:139], v[20:23]\n"
    "ds_read_b64_tr_b16 a[228:229], v117 offset:16384\n"
    "v_add_f32 v132, v132, v69\n"
    "v_mfma_f32_16x16x32_bf16 v[28:31], a[216:219], a[152:155], v[28:31]\n"
    "ds_read_b64_tr_b16 a[230:231], v117 offset:20480\n"
    "v_cvt_pk_bf16_f32 v104, v64, v65\n"
    "v_mfma_f32_16x16x32_bf16 v[16:19], a[204:207], a[140:143], v[16:19]\n"
    "ds_read_b64_tr_b16 a[232:233], v118 offset:16384\n"
    "v_exp_f32 v79, v47\n"
    "v_mfma_f32_16x16x32_bf16 v[24:27], a[204:207], a[156:159], v[24:27]\n"
    "ds_read_b64_tr_b16 a[234:235], v118 offset:20480\n"
    "v_cvt_pk_bf16_f32 v105, v66, v67\n"
    "v_mfma_f32_16x16x32_bf16 v[20:23], a[220:223], a[140:143], v[20:23]\n"
    "ds_read_b64_tr_b16 a[236:237], v119 offset:16384\n"
    "v_cvt_pk_bf16_f32 v106, v68, v69\n"
    "v_mfma_f32_16x16x32_bf16 v[28:31], a[220:223], a[156:159], v[28:31]\n"
    "ds_read_b64_tr_b16 a[238:239], v119 offset:20480\n"
    "s_waitcnt lgkmcnt(8)\n"
    "v_mfma_f32_16x16x32_bf16 a[0:3], a[240:243], v[96:99], a[0:3]\n"
    "s_add_u32 m0, s71, 8192\n"
    "v_add_f32 v134, v134, v130\n"
    "v_cvt_pk_bf16_f32 v107, v70, v71\n"
    "v_add_f32 v135, v135, v131\n"
    "v_mfma_f32_16x16x32_bf16 a[16:19], a[240:243], v[100:103], a[16:19]\n"
    "buffer_load_dwordx4 v128, s[44:47], s67 offen offset:0 nt lds\n"
    "v_add_f32 v133, v133, v76\n"
    "v_cvt_pk_bf16_f32 v108, v72, v73\n"
    "v_mfma_f32_16x16x32_bf16 a[4:7], a[244:247], v[96:99], a[4:7]\n"
    "buffer_load_dwordx4 v129, s[44:47], s67 offen offset:1024 nt lds\n"
    "v_add_f32 v133, v133, v77\n"
    "v_add_f32 v132, v132, v70\n"
    "v_mfma_f32_16x16x32_bf16 a[20:23], a[244:247], v[100:103], a[20:23]\n"
    "v_exp_f32 v48, v16\n"
    "v_mfma_f32_16x16x32_bf16 a[8:11], a[248:251], v[96:99], a[8:11]\n"
    "v_exp_f32 v49, v17\n"
    "v_mfma_f32_16x16x32_bf16 a[24:27], a[248:251], v[100:103], a[24:27]\n"
    "v_exp_f32 v50, v18\n"
    "v_cvt_pk_bf16_f32 v109, v74, v75\n"
    "v_mfma_f32_16x16x32_bf16 a[12:15], a[252:255], v[96:99], a[12:15]\n"
    "v_exp_f32 v51, v19\n"
    "v_add_f32 v133, v133, v78\n"
    "v_mfma_f32_16x16x32_bf16 a[28:31], a[252:255], v[100:103], a[28:31]\n"
    "s_waitcnt lgkmcnt(0)\n"
    "v_exp_f32 v52, v20\n"
    "v_mfma_f32_16x16x32_bf16 v[192:195], a[224:227], v[96:99], v[192:195]\n"
    "buffer_load_dwordx4 v128, s[44:47], s67 offen offset:2048 nt lds\n"
    "v_exp_f32 v53, v21\n"
    "v_mfma_f32_16x16x32_bf16 v[208:211], a[224:227], v[100:103], v[208:211]\n"
    "v_add_f32 v132, v132, v71\n"
    "v_cvt_pk_bf16_f32 v110, v76, v77\n"
    "v_mfma_f32_16x16x32_bf16 v[196:199], a[228:231], v[96:99], v[196:199]\n"
    "v_exp_f32 v54, v22\n"
    "v_add_f32 v133, v133, v79\n"
    "v_mfma_f32_16x16x32_bf16 v[212:215], a[228:231], v[100:103], v[212:215]\n"
    "v_exp_f32 v55, v23\n"
    "v_mfma_f32_16x16x32_bf16 v[200:203], a[232:235], v[96:99], v[200:203]\n"
    "buffer_load_dwordx4 v129, s[44:47], s67 offen offset:3072 nt lds\n"
    "v_max_f32 v15, v132, v133\n"
    "s_add_u32 s67, s67, 16384\n"
    "v_mfma_f32_16x16x32_bf16 v[216:219], a[232:235], v[100:103], v[216:219]\n"
    "v_exp_f32 v56, v24\n"
    "v_cvt_pk_bf16_f32 v111, v78, v79\n"
    "v_mfma_f32_16x16x32_bf16 v[204:207], a[236:239], v[96:99], v[204:207]\n"
    "v_cmp_nge_f32 vcc, s76, v15\n"
    "v_exp_f32 v57, v25\n"
    "v_mfma_f32_16x16x32_bf16 v[220:223], a[236:239], v[100:103], v[220:223]\n"
    "v_exp_f32 v58, v26\n"
    "s_cbranch_vccnz .Lrare_b1\n"
    ".Lback_b1:\n"
    "v_mfma_f32_16x16x32_bf16 v[32:35], a[192:195], a[160:163], v[88:91]\n"
    "s_add_u32 m0, s71, 32768\n"
    "v_exp_f32 v59, v27\n"
    "v_mfma_f32_16x16x32_bf16 v[40:43], a[192:195], a[176:179], v[92:95]\n"
    "buffer_load_dwordx4 v124, s[40:43], s66 offen offset:0 nt lds\n"
    "v_add_f32 v136, v136, v132\n"
    "v_mfma_f32_16x16x32_bf16 v[36:39], a[208:211], a[160:163], v[88:91]\n"
    "v_cvt_pk_bf16_f32 v96, v48, v49\n"
    "v_add_f32 v137, v137, v133\n"
    "v_mfma_f32_16x16x32_bf16 v[44:47], a[208:211], a[176:179], v[92:95]\n"
    "v_exp_f32 v60, v28\n"
    "v_mfma_f32_16x16x32_bf16 v[32:35], a[196:199], a[164:167], v[32:35]\n"
    "v_add_f32 v130, v48, v49\n"
    "v_cvt_pk_bf16_f32 v97, v50, v51\n"
    "v_mfma_f32_16x16x32_bf16 v[40:43], a[196:199], a[180:183], v[40:43]\n"
    "v_exp_f32 v61, v29\n"
    "v_mfma_f32_16x16x32_bf16 v[36:39], a[212:215], a[164:167], v[36:39]\n"
    "buffer_load_dwordx4 v125, s[40:43], s66 offen offset:1024 nt lds\n"
    "v_add_f32 v131, v56, v57\n"
    "v_mfma_f32_16x16x32_bf16 v[44:47], a[212:215], a[180:183], v[44:47]\n"
    "v_add_f32 v130, v130, v50\n"
    "v_exp_f32 v62, v30\n"
    "v_mfma_f32_16x16x32_bf16 v[32:35], a[200:203], a[168:171], v[32:35]\n"
    "v_cvt_pk_bf16_f32 v98, v52, v53\n"
    "v_add_f32 v131, v131, v58\n"
    "v_mfma_f32_16x16x32_bf16 v[40:43], a[200:203], a[184:187], v[40:43]\n"
    "v_exp_f32 v63, v31\n"
    "v_mfma_f32_16x16x32_bf16 v[36:39], a[216:219], a[168:171], v[36:39]\n"
    "buffer_load_dwordx4 v126, s[40:43], s66 offen offset:2048 nt lds\n"
    "v_add_f32 v130, v130, v51\n"
    "v_mfma_f32_16x16x32_bf16 v[44:47], a[216:219], a[184:187], v[44:47]\n"
    "v_add_f32 v131, v131, v59\n"
    "v_cvt_pk_bf16_f32 v99, v54, v55\n"
    "v_mfma_f32_16x16x32_bf16 v[32:35], a[204:207], a[172:175], v[32:35]\n"
    "v_add_f32 v130, v130, v52\n"
    "v_add_f32 v131, v131, v60\n"
    "v_mfma_f32_16x16x32_bf16 v[40:43], a[204:207], a[188:191], v[40:43]\n"
    "v_cvt_pk_bf16_f32 v100, v56, v57\n"
    "v_add_f32 v130, v130, v53\n"
    "v_mfma_f32_16x16x32_bf16 v[36:39], a[220:223], a[172:175], v[36:39]\n"
    "buffer_load_dwordx4 v127, s[40:43], s66 offen offset:3072 nt lds\n"
    "s_add_u32 s66, s66, 16384\n"
    "v_add_f32 v131, v131, v61\n"
    "v_cvt_pk_bf16_f32 v101, v58, v59\n"
    "v_mfma_f32_16x16x32_bf16 v[44:47], a[220:223], a[188:191], v[44:47]\n"
    "v_add_f32 v130, v130, v54\n"
    "v_add_f32 v131, v131, v62\n"
    "v_mfma_f32_16x16x32_bf16 a[32:35], a[240:243], v[104:107], a[32:35]\n"
    "ds_read_b128 a[192:195], v112 offset:49152\n"
    "v_add_f32 v130, v130, v55\n"
    "v_mfma_f32_16x16x32_bf16 a[48:51], a[240:243], v[108:111], a[48:51]\n"
    "ds_read_b128 a[208:211], v112 offset:53248\n"
    "v_cvt_pk_bf16_f32 v102, v60, v61\n"
    "v_mfma_f32_16x16x32_bf16 a[36:39], a[244:247], v[104:107], a[36:39]\n"
    "ds_read_b128 a[196:199], v113 offset:49152\n"
    "v_add_f32 v131, v131, v63\n"
    "v_mfma_f32_16x16x32_bf16 a[52:55], a[244:247], v[108:111], a[52:55]\n"
    "v_exp_f32 v64, v32\n"
    "v_mfma_f32_16x16x32_bf16 a[40:43], a[248:251], v[104:107], a[40:43]\n"
    "ds_read_b128 a[212:215], v113 offset:53248\n"
    "v_exp_f32 v65, v33\n"
    "v_mfma_f32_16x16x32_bf16 a[56:59], a[248:251], v[108:111], a[56:59]\n"
    "ds_read_b128 a[200:203], v114 offset:49152\n"
    "v_exp_f32 v66, v34\n"
    "v_mfma_f32_16x16x32_bf16 a[44:47], a[252:255], v[104:107], a[44:47]\n"
    "ds_read_b128 a[216:219], v114 offset:53248\n"
    "v_exp_f32 v67, v35\n"
    "v_mfma_f32_16x16x32_bf16 a[60:63], a[252:255], v[108:111], a[60:63]\n"
    "v_exp_f32 v68, v36\n"
    "v_mfma_f32_16x16x32_bf16 v[224:227], a[224:227], v[104:107], v[224:227]\n"
    "ds_read_b128 a[204:207], v115 offset:49152\n"
    "v_max_f32 v15, v130, v131\n"
    "v_mfma_f32_16x16x32_bf16 v[240:243], a[224:227], v[108:111], v[240:243]\n"
    "ds_read_b128 a[220:223], v115 offset:53248\n"
    "v_cvt_pk_bf16_f32 v103, v62, v63\n"
    "v_mfma_f32_16x16x32_bf16 v[228:231], a[228:231], v[104:107], v[228:231]\n"
    "v_exp_f32 v69, v37\n"
    "v_mfma_f32_16x16x32_bf16 v[244:247], a[228:231], v[108:111], v[244:247]\n"
    "v_exp_f32 v70, v38\n"
    "v_mfma_f32_16x16x32_bf16 v[232:235], a[232:235], v[104:107], v[232:235]\n"
    "v_exp_f32 v71, v39\n"
    "v_cmp_nge_f32 vcc, s76, v15\n"
    "v_mfma_f32_16x16x32_bf16 v[248:251], a[232:235], v[108:111], v[248:251]\n"
    "s_memrealtime s[34:35]\n"
    "v_exp_f32 v72, v40\n"
    "v_mfma_f32_16x16x32_bf16 v[236:239], a[236:239], v[104:107], v[236:239]\n"
    "v_exp_f32 v73, v41\n"
    "v_exp_f32 v74, v42\n"
    "v_mfma_f32_16x16x32_bf16 v[252:255], a[236:239], v[108:111], v[252:255]\n"
    "v_exp_f32 v75, v43\n"
    "s_cbranch_vccnz .Lrare_a2\n"
    ".Lback_a2:\n"
    "s_waitcnt vmcnt(16) lgkmcnt(0)\n"
    "s_sub_u32 s28, s34, s80\n"
    "s_cmp_lt_i32 s28, 0\n"
    "s_cbranch_scc1 .Lwait_2\n"
    ".Lgo_2:\n"
    "s_add_u32 s80, s34, s77\n"
    "s_barrier\n"
    "v_mfma_f32_16x16x32_bf16 v[16:19], a[192:195], a[128:131], v[80:83]\n"
    "ds_read_b64_tr_b16 a[240:241], v120 offset:32768\n"
    "v_exp_f32 v76, v44\n"
    "v_mfma_f32_16x16x32_bf16 v[24:27], a[192:195], a[144:147], v[84:87]\n"
    "ds_read_b64_tr_b16 a[242:243], v120 offset:36864\n"
    "v_add_f32 v132, v64, v65\n"
    "v_mfma_f32_16x16x32_bf16 v[20:23], a[208:211], a[128:131], v[80:83]\n"
    "ds_read_b64_tr_b16 a[244:245], v121 offset:32768\n"
    "v_add_f32 v133, v72, v73\n"
    "v_mfma_f32_16x16x32_bf16 v[28:31], a[208:211], a[144:147], v[84:87]\n"
    "ds_read_b64_tr_b16 a[246:247], v121 offset:36864\n"
    "v_add_f32 v132, v132, v66\n"
    "v_mfma_f32_16x16x32_bf16 v[16:19], a[196:199], a[132:135], v[16:19]\n"
    "ds_read_b64_tr_b16 a[248:249], v122 offset:32768\n"
    "v_exp_f32 v77, v45\n"
    "v_mfma_f32_16x16x32_bf16 v[24:27], a[196:199], a[148:151], v[24:27]\n"
    "ds_read_b64_tr_b16 a[250:251], v122 offset:36864\n"
    "v_add_f32 v133, v133, v74\n"
    "v_mfma_f32_16x16x32_bf16 v[20:23], a[212:215], a[132:135], v[20:23]\n"
    "ds_read_b64_tr_b16 a[252:253], v123 offset:32768\n"
    "v_add_f32 v132, v132, v67\n"
    "v_mfma_f32_16x16x32_bf16 v[28:31], a[212:215], a[148:151], v[28:31]\n"
    "ds_read_b64_tr_b16 a[254:255], v123 offset:36864\n"
    "v_add_f32 v133, v133, v75\n"
    "v_mfma_f32_16x16x32_bf16 v[16:19], a[200:203], a[136:139], v[16:19]\n"
    "ds_read_b64_tr_b16 a[224:225], v116 offset:32768\n"
    "v_exp_f32 v78, v46\n"
    "v_mfma_f32_16x16x32_bf16 v[24:27], a[200:203], a[152:155], v[24:27]\n"
    "ds_read_b64_tr_b16 a[226:227], v116 offset:36864\n"
    "v_add_f32 v132, v132, v68\n"
    "v_mfma_f32_16x16x32_bf16 v[20:23], a[216:219], a[136:139], v[20:23]\n"
    "ds_read_b64_tr_b16 a[228:229], v117 offset:32768\n"
    "v_add_f32 v132, v132, v69\n"
    "v_mfma_f32_16x16x32_bf16 v[28:31], a[216:219], a[152:155], v[28:31]\n"
    "ds_read_b64_tr_b16 a[230:231], v117 offset:36864\n"
    "v_cvt_pk_bf16_f32 v104, v64, v65\n"
    "v_mfma_f32_16x16x32_bf16 v[16:19], a[204:207], a[140:143], v[16:19]\n"
    "ds_read_b64_tr_b16 a[232:233], v118 offset:32768\n"
    "v_exp_f32 v79, v47\n"
    "v_mfma_f32_16x16x32_bf16 v[24:27], a[204:207], a[156:159], v[24:27]\n"
    "ds_read_b64_tr_b16 a[234:235], v118 offset:36864\n"
    "v_cvt_pk_bf16_f32 v105, v66, v67\n"
    "v_mfma_f32_16x16x32_bf16 v[20:23], a[220:223], a[140:143], v[20:23]\n"
    "ds_read_b64_tr_b16 a[236:237], v119 offset:32768\n"
    "v_cvt_pk_bf16_f32 v106, v68, v69\n"
    "v_mfma_f32_16x16x32_bf16 v[28:31], a[220:223], a[156:159], v[28:31]\n"
    "ds_read_b64_tr_b16 a[238:239], v119 offset:36864\n"
    "s_waitcnt lgkmcnt(8)\n"
    "v_mfma_f32_16x16x32_bf16 a[0:3], a[240:243], v[96:99], a[0:3]\n"
    "s_add_u32 m0, s71, 24576\n"
    "v_add_f32 v134, v134, v130\n"
    "v_cvt_pk_bf16_f32 v107, v70, v71\n"
    "v_add_f32 v135, v135, v131\n"
    "v_mfma_f32_16x16x32_bf16 a[16:19], a[240:243], v[100:103], a[16:19]\n"
    "buffer_load_dwordx4 v128, s[44:47], s67 offen offset:0 nt lds\n"
    "v_add_f32 v133, v133, v76\n"
    "v_cvt_pk_bf16_f32 v108, v72, v73\n"
    "v_mfma_f32_16x16x32_bf16 a[4:7], a[244:247], v[96:99], a[4:7]\n"
    "buffer_load_dwordx4 v129, s[44:47], s67 offen offset:1024 nt lds\n"
    "v_add_f32 v133, v133, v77\n"
    "v_add_f32 v132, v132, v70\n"
    "v_mfma_f32_16x16x32_bf16 a[20:23], a[244:247], v[100:103], a[20:23]\n"
    "v_exp_f32 v48, v16\n"
    "v_mfma_f32_16x16x32_bf16 a[8:11], a[248:251], v[96:99], a[8:11]\n"
    "v_exp_f32 v49, v17\n"
    "v_mfma_f32_16x16x32_bf16 a[24:27], a[248:251], v[100:103], a[24:27]\n"
    "v_exp_f32 v50, v18\n"
    "v_cvt_pk_bf16_f32 v109, v74, v75\n"
    "v_mfma_f32_16x16x32_bf16 a[12:15], a[252:255], v[96:99], a[12:15]\n"
    "v_exp_f32 v51, v19\n"
    "v_add_f32 v133, v133, v78\n"
    "v_mfma_f32_16x16x32_bf16 a[28:31], a[252:255], v[100:103], a[28:31]\n"
    "s_waitcnt lgkmcnt(0)\n"
    "v_exp_f32 v52, v20\n"
    "v_mfma_f32_16x16x32_bf16 v[192:195], a[224:227], v[96:99], v[192:195]\n"
    "buffer_load_dwordx4 v128, s[44:47], s67 offen offset:2048 nt lds\n"
    "v_exp_f32 v53, v21\n"
    "v_mfma_f32_16x16x32_bf16 v[208:211], a[224:227], v[100:103], v[208:211]\n"
    "v_add_f32 v132, v132, v71\n"
    "v_cvt_pk_bf16_f32 v110, v76, v77\n"
    "v_mfma_f32_16x16x32_bf16 v[196:199], a[228:231], v[96:99], v[196:199]\n"
    "v_exp_f32 v54, v22\n"
    "v_add_f32 v133, v133, v79\n"
    "v_mfma_f32_16x16x32_bf16 v[212:215], a[228:231], v[100:103], v[212:215]\n"
    "v_exp_f32 v55, v23\n"
    "v_mfma_f32_16x16x32_bf16 v[200:203], a[232:235], v[96:99], v[200:203]\n"
    "buffer_load_dwordx4 v129, s[44:47], s67 offen offset:3072 nt lds\n"
    "v_max_f32 v15, v132, v133\n"
    "s_add_u32 s67, s67, 16384\n"
    "v_mfma_f32_16x16x32_bf16 v[216:219], a[232:235], v[100:103], v[216:219]\n"
    "v_exp_f32 v56, v24\n"
    "v_cvt_pk_bf16_f32 v111, v78, v79\n"
    "v_mfma_f32_16x16x32_bf16 v[204:207], a[236:239], v[96:99], v[204:207]\n"
    "v_cmp_nge_f32 vcc, s76, v15\n"
    "v_exp_f32 v57, v25\n"
    "v_mfma_f32_16x16x32_bf16 v[220:223], a[236:239], v[100:103], v[220:223]\n"
    "v_exp_f32 v58, v26\n"
    "s_cbranch_vccnz .Lrare_b2\n"
    ".Lback_b2:\n"
    "v_mfma_f32_16x16x32_bf16 v[32:35], a[192:195], a[160:163], v[88:91]\n"
    "s_add_u32 m0, s71, 49152\n"
    "v_exp_f32 v59, v27\n"
    "v_mfma_f32_16x16x32_bf16 v[40:43], a[192:195], a[176:179], v[92:95]\n"
    "buffer_load_dwordx4 v124, s[40:43], s66 offen offset:0 nt lds\n"
    "v_add_f32 v136, v136, v132\n"
    "v_mfma_f32_16x16x32_bf16 v[36:39], a[208:211], a[160:163], v[88:91]\n"
    "v_cvt_pk_bf16_f32 v96, v48, v49\n"
    "v_add_f32 v137, v137, v133\n"
    "v_mfma_f32_16x16x32_bf16 v[44:47], a[208:211], a[176:179], v[92:95]\n"
    "v_exp_f32 v60, v28\n"
    "v_mfma_f32_16x16x32_bf16 v[32:35], a[196:199], a[164:167], v[32:35]\n"
    "v_add_f32 v130, v48, v49\n"
    "v_cvt_pk_bf16_f32 v97, v50, v51\n"
    "v_mfma_f32_16x16x32_bf16 v[40:43], a[196:199], a[180:183], v[40:43]\n"
    "v_exp_f32 v61, v29\n"
    "v_mfma_f32_16x16x32_bf16 v[36:39], a[212:215], a[164:167], v[36:39]\n"
    "buffer_load_dwordx4 v125, s[40:43], s66 offen offset:1024 nt lds\n"
    "v_add_f32 v131, v56, v57\n"
    "v_mfma_f32_16x16x32_bf16 v[44:47], a[212:215], a[180:183], v[44:47]\n"
    "v_add_f32 v130, v130, v50\n"
    "v_exp_f32 v62, v30\n"
    "v_mfma_f32_16x16x32_bf16 v[32:35], a[200:203], a[168:171], v[32:35]\n"
    "v_cvt_pk_bf16_f32 v98, v52, v53\n"
    "v_add_f32 v131, v131, v58\n"
    "v_mfma_f32_16x16x32_bf16 v[40:43], a[200:203], a[184:187], v[40:43]\n"
    "v_exp_f32 v63, v31\n"
    "v_mfma_f32_16x16x32_bf16 v[36:39], a[216:219], a[168:171], v[36:39]\n"
    "buffer_load_dwordx4 v126, s[40:43], s66 offen offset:2048 nt lds\n"
    "v_add_f32 v130, v130, v51\n"
    "v_mfma_f32_16x16x32_bf16 v[44:47], a[216:219], a[184:187], v[44:47]\n"
    "v_add_f32 v131, v131, v59\n"
    "v_cvt_pk_bf16_f32 v99, v54, v55\n"
    "v_mfma_f32_16x16x32_bf16 v[32:35], a[204:207], a[172:175], v[32:35]\n"
    "v_add_f32 v130, v130, v52\n"
    "v_add_f32 v131, v131, v60\n"
    "v_mfma_f32_16x16x32_bf16 v[40:43], a[204:207], a[188:191], v[40:43]\n"
    "v_cvt_pk_bf16_f32 v100, v56, v57\n"
    "v_add_f32 v130, v130, v53\n"
    "v_mfma_f32_16x16x32_bf16 v[36:39], a[220:223], a[172:175], v[36:39]\n"
    "buffer_load_dwordx4 v127, s[40:43], s66 offen offset:3072 nt lds\n"
    "s_add_u32 s66, s66, 16384\n"
    "v_add_f32 v131, v131, v61\n"
    "v_cvt_pk_bf16_f32 v101, v58, v59\n"
    "v_mfma_f32_16x16x32_bf16 v[44:47], a[220:223], a[188:191], v[44:47]\n"
    "v_add_f32 v130, v130, v54\n"
    "v_add_f32 v131, v131, v62\n"
    "v_mfma_f32_16x16x32_bf16 a[32:35], a[240:243], v[104:107], a[32:35]\n"
    "ds_read_b128 a[192:195], v112 offset:0\n"
    "v_add_f32 v130, v130, v55\n"
    "v_mfma_f32_16x16x32_bf16 a[48:51], a[240:243], v[108:111], a[48:51]\n"
    "ds_read_b128 a[208:211], v112 offset:4096\n"
    "v_cvt_pk_bf16_f32 v102, v60, v61\n"
    "v_mfma_f32_16x16x32_bf16 a[36:39], a[244:247], v[104:107], a[36:39]\n"
    "ds_read_b128 a[196:199], v113 offset:0\n"
    "v_add_f32 v131, v131, v63\n"
    "v_mfma_f32_16x16x32_bf16 a[52:55], a[244:247], v[108:111], a[52:55]\n"
    "v_exp_f32 v64, v32\n"
    "v_mfma_f32_16x16x32_bf16 a[40:43], a[248:251], v[104:107], a[40:43]\n"
    "ds_read_b128 a[212:215], v113 offset:4096\n"
    "v_exp_f32 v65, v33\n"
    "v_mfma_f32_16x16x32_bf16 a[56:59], a[248:251], v[108:111], a[56:59]\n"
    "ds_read_b128 a[200:203], v114 offset:0\n"
    "v_exp_f32 v66, v34\n"
    "v_mfma_f32_16x16x32_bf16 a[44:47], a[252:255], v[104:107], a[44:47]\n"
    "ds_read_b128 a[216:219], v114 offset:4096\n"
    "v_exp_f32 v67, v35\n"
    "v_mfma_f32_16x16x32_bf16 a[60:63], a[252:255], v[108:111], a[60:63]\n"
    "v_exp_f32 v68, v36\n"
    "v_mfma_f32_16x16x32_bf16 v[224:227], a[224:227], v[104:107], v[224:227]\n"
    "ds_read_b128 a[204:207], v115 offset:0\n"
    "v_max_f32 v15, v130, v131\n"
    "v_mfma_f32_16x16x32_bf16 v[240:243], a[224:227], v[108:111], v[240:243]\n"
    "ds_read_b128 a[220:223], v115 offset:4096\n"
    "v_cvt_pk_bf16_f32 v103, v62, v63\n"
    "v_mfma_f32_16x16x32_bf16 v[228:231], a[228:231], v[104:107], v[228:231]\n"
    "v_exp_f32 v69, v37\n"
    "v_mfma_f32_16x16x32_bf16 v[244:247], a[228:231], v[108:111], v[244:247]\n"
    "v_exp_f32 v70, v38\n"
    "v_mfma_f32_16x16x32_bf16 v[232:235], a[232:235], v[104:107], v[232:235]\n"
    "v_exp_f32 v71, v39\n"
    "v_cmp_nge_f32 vcc, s76, v15\n"
    "v_mfma_f32_16x16x32_bf16 v[248:251], a[232:235], v[108:111], v[248:251]\n"
    "s_memrealtime s[34:35]\n"
    "v_exp_f32 v72, v40\n"
    "v_mfma_f32_16x16x32_bf16 v[236:239], a[236:239], v[104:107], v[236:239]\n"
    "v_exp_f32 v73, v41\n"
    "v_exp_f32 v74, v42\n"
    "v_mfma_f32_16x16x32_bf16 v[252:255], a[236:239], v[108:111], v[252:255]\n"
    "v_exp_f32 v75, v43\n"
    "s_cmp_eq_u32 s65, 0\n"
    "s_cbranch_scc1 .Lattn_last\n"
    "s_sub_u32 s65, s65, 1\n"
    "s_cmp_ge_u32 s65, s78\n"
    "s_cselect_b32 s77, s77, 0\n"
    "s_cbranch_vccnz .Lrare_a3\n"
    ".Lback_a3:\n"
    "s_waitcnt vmcnt(16) lgkmcnt(0)\n"
    "s_sub_u32 s28, s34, s80\n"
    "s_cmp_lt_i32 s28, 0\n"
    "s_cbranch_scc1 .Lwait_3\n"
    ".Lgo_3:\n"
    "s_add_u32 s80, s34, s77\n"
    "s_barrier\n"
    "v_mfma_f32_16x16x32_bf16 v[16:19], a[192:195], a[128:131], v[80:83]\n"
    "ds_read_b64_tr_b16 a[240:241], v120 offset:49152\n"
    "v_exp_f32 v76, v44\n"
    "v_mfma_f32_16x16x32_bf16 v[24:27], a[192:195], a[144:147], v[84:87]\n"
    "ds_read_b64_tr_b16 a[242:243], v120 offset:53248\n"
    "v_add_f32 v132, v64, v65\n"
    "v_mfma_f32_16x16x32_bf16 v[20:23], a[208:211], a[128:131], v[80:83]\n"
    "ds_read_b64_tr_b16 a[244:245], v121 offset:49152\n"
    "v_add_f32 v133, v72, v73\n"
    "v_mfma_f32_16x16x32_bf16 v[28:31], a[208:211], a[144:147], v[84:87]\n"
    "ds_read_b64_tr_b16 a[246:247], v121 offset:53248\n"
    "v_add_f32 v132, v132, v66\n"
    "v_mfma_f32_16x16x32_bf16 v[16:19], a[196:199], a[132:135], v[16:19]\n"
    "ds_read_b64_tr_b16 a[248:249], v122 offset:49152\n"
    "v_exp_f32 v77, v45\n"
    "v_mfma_f32_16x16x32_bf16 v[24:27], a[196:199], a[148:151], v[24:27]\n"
    "ds_read_b64_tr_b16 a[250:251], v122 offset:53248\n"
    "v_add_f32 v133, v133, v74\n"
    "v_mfma_f32_16x16x32_bf16 v[20:23], a[212:215], a[132:135], v[20:23]\n"
    "ds_read_b64_tr_b16 a[252:253], v123 offset:49152\n"
    "v_add_f32 v132, v132, v67\n"
    "v_mfma_f32_16x16x32_bf16 v[28:31], a[212:215], a[148:151], v[28:31]\n"
    "ds_read_b64_tr_b16 a[254:255], v123 offset:53248\n"
    "v_add_f32 v133, v133, v75\n"
    "v_mfma_f32_16x16x32_bf16 v[16:19], a[200:203], a[136:139], v[16:19]\n"
    "ds_read_b64_tr_b16 a[224:225], v116 offset:49152\n"
    "v_exp_f32 v78, v46\n"
    "v_mfma_f32_16x16x32_bf16 v[24:27], a[200:203], a[152:155], v[24:27]\n"
    "ds_read_b64_tr_b16 a[226:227], v116 offset:53248\n"
    "v_add_f32 v132, v132, v68\n"
    "v_mfma_f32_16x16x32_bf16 v[20:23], a[216:219], a[136:139], v[20:23]\n"
    "ds_read_b64_tr_b16 a[228:229], v117 offset:49152\n"
    "v_add_f32 v132, v132, v69\n"
    "v_mfma_f32_16x16x32_bf16 v[28:31], a[216:219], a[152:155], v[28:31]\n"
    "ds_read_b64_tr_b16 a[230:231], v117 offset:53248\n"
    "v_cvt_pk_bf16_f32 v104, v64, v65\n"
    "v_mfma_f32_16x16x32_bf16 v[16:19], a[204:207], a[140:143], v[16:19]\n"
    "ds_read_b64_tr_b16 a[232:233], v118 offset:49152\n"
    "v_exp_f32 v79, v47\n"
    "v_mfma_f32_16x16x32_bf16 v[24:27], a[204:207], a[156:159], v[24:27]\n"
    "ds_read_b64_tr_b16 a[234:235], v118 offset:53248\n"
    "v_cvt_pk_bf16_f32 v105, v66, v67\n"
    "v_mfma_f32_16x16x32_bf16 v[20:23], a[220:223], a[140:143], v[20:23]\n"
    "ds_read_b64_tr_b16 a[236:237], v119 offset:49152\n"
    "v_cvt_pk_bf16_f32 v106, v68, v69\n"
    "v_mfma_f32_16x16x32_bf16 v[28:31], a[220:223], a[156:159], v[28:31]\n"
    "ds_read_b64_tr_b16 a[238:239], v119 offset:53248\n"
    "s_waitcnt lgkmcnt(8)\n"
    "v_mfma_f32_16x16x32_bf16 a[0:3], a[240:243], v[96:99], a[0:3]\n"
    "s_add_u32 m0, s71, 40960\n"
    "v_add_f32 v134, v134, v130\n"
    "v_cvt_pk_bf16_f32 v107, v70, v71\n"
    "v_add_f32 v135, v135, v131\n"
    "v_mfma_f32_16x16x32_bf16 a[16:19], a[240:243], v[100:103], a[16:19]\n"
    "buffer_load_dwordx4 v128, s[44:47], s67 offen offset:0 nt lds\n"
    "v_add_f32 v133, v133, v76\n"
    "v_cvt_pk_bf16_f32 v108, v72, v73\n"
    "v_mfma_f32_16x16x32_bf16 a[4:7], a[244:247], v[96:99], a[4:7]\n"
    "buffer_load_dwordx4 v129, s[44:47], s67 offen offset:1024 nt lds\n"
    "v_add_f32 v133, v133, v77\n"
    "v_add_f32 v132, v132, v70\n"
    "v_mfma_f32_16x16x32_bf16 a[20:23], a[244:247], v[100:103], a[20:23]\n"
    "v_exp_f32 v48, v16\n"
    "v_mfma_f32_16x16x32_bf16 a[8:11], a[248:251], v[96:99], a[8:11]\n"
    "v_exp_f32 v49, v17\n"
    "v_mfma_f32_16x16x32_bf16 a[24:27], a[248:251], v[100:103], a[24:27]\n"
    "v_exp_f32 v50, v18\n"
    "v_cvt_pk_bf16_f32 v109, v74, v75\n"
    "v_mfma_f32_16x16x32_bf16 a[12:15], a[252:255], v[96:99], a[12:15]\n"
    "v_exp_f32 v51, v19\n"
    "v_add_f32 v133, v133, v78\n"
    "v_mfma_f32_16x16x32_bf16 a[28:31], a[252:255], v[100:103], a[28:31]\n"
    "s_waitcnt lgkmcnt(0)\n"
    "v_exp_f32 v52, v20\n"
    "v_mfma_f32_16x16x32_bf16 v[192:195], a[224:227], v[96:99], v[192:195]\n"
    "buffer_load_dwordx4 v128, s[44:47], s67 offen offset:2048 nt lds\n"
    "v_exp_f32 v53, v21\n"
    "v_mfma_f32_16x16x32_bf16 v[208:211], a[224:227], v[100:103], v[208:211]\n"
    "v_add_f32 v132, v132, v71\n"
    "v_cvt_pk_bf16_f32 v110, v76, v77\n"
    "v_mfma_f32_16x16x32_bf16 v[196:199], a[228:231], v[96:99], v[196:199]\n"
    "v_exp_f32 v54, v22\n"
    "v_add_f32 v133, v133, v79\n"
    "v_mfma_f32_16x16x32_bf16 v[212:215], a[228:231], v[100:103], v[212:215]\n"
    "v_exp_f32 v55, v23\n"
    "v_mfma_f32_16x16x32_bf16 v[200:203], a[232:235], v[96:99], v[200:203]\n"
    "buffer_load_dwordx4 v129, s[44:47], s67 offen offset:3072 nt lds\n"
    "v_max_f32 v15, v132, v133\n"
    "s_add_u32 s67, s67, 16384\n"
    "v_mfma_f32_16x16x32_bf16 v[216:219], a[232:235], v[100:103], v[216:219]\n"
    "v_exp_f32 v56, v24\n"
    "v_cvt_pk_bf16_f32 v111, v78, v79\n"
    "v_mfma_f32_16x16x32_bf16 v[204:207], a[236:239], v[96:99], v[204:207]\n"
    "v_cmp_nge_f32 vcc, s76, v15\n"
    "v_exp_f32 v57, v25\n"
    "v_mfma_f32_16x16x32_bf16 v[220:223], a[236:239], v[100:103], v[220:223]\n"
    "v_exp_f32 v58, v26\n"
    "s_cbranch_vccnz .Lrare_b3\n"
    ".Lback_b3:\n"
    "v_mfma_f32_16x16x32_bf16 v[32:35], a[192:195], a[160:163], v[88:91]\n"
    "s_add_u32 m0, s71, 0\n"
    "v_exp_f32 v59, v27\n"
    "v_mfma_f32_16x16x32_bf16 v[40:43], a[192:195], a[176:179], v[92:95]\n"
    "buffer_load_dwordx4 v124, s[40:43], s66 offen offset:0 nt lds\n"
    "v_add_f32 v136, v136, v132\n"
    "v_mfma_f32_16x16x32_bf16 v[36:39], a[208:211], a[160:163], v[88:91]\n"
    "v_cvt_pk_bf16_f32 v96, v48, v49\n"
    "v_add_f32 v137, v137, v133\n"
    "v_mfma_f32_16x16x32_bf16 v[44:47], a[208:211], a[176:179], v[92:95]\n"
    "v_exp_f32 v60, v28\n"
    "v_mfma_f32_16x16x32_bf16 v[32:35], a[196:199], a[164:167], v[32:35]\n"
    "v_add_f32 v130, v48, v49\n"
    "v_cvt_pk_bf16_f32 v97, v50, v51\n"
    "v_mfma_f32_16x16x32_bf16 v[40:43], a[196:199], a[180:183], v[40:43]\n"
    "v_exp_f32 v61, v29\n"
    "v_mfma_f32_16x16x32_bf16 v[36:39], a[212:215], a[164:167], v[36:39]\n"
    "buffer_load_dwordx4 v125, s[40:43], s66 offen offset:1024 nt lds\n"
    "v_add_f32 v131, v56, v57\n"
    "v_mfma_f32_16x16x32_bf16 v[44:47], a[212:215], a[180:183], v[44:47]\n"
    "v_add_f32 v130, v130, v50\n"
    "v_exp_f32 v62, v30\n"
    "v_mfma_f32_16x16x32_bf16 v[32:35], a[200:203], a[168:171], v[32:35]\n"
    "v_cvt_pk_bf16_f32 v98, v52, v53\n"
    "v_add_f32 v131, v131, v58\n"
    "v_mfma_f32_16x16x32_bf16 v[40:43], a[200:203], a[184:187], v[40:43]\n"
    "v_exp_f32 v63, v31\n"
    "v_mfma_f32_16x16x32_bf16 v[36:39], a[216:219], a[168:171], v[36:39]\n"
    "buffer_load_dwordx4 v126, s[40:43], s66 offen offset:2048 nt lds\n"
    "v_add_f32 v130, v130, v51\n"
    "v_mfma_f32_16x16x32_bf16 v[44:47], a[216:219], a[184:187], v[44:47]\n"
    "v_add_f32 v131, v131, v59\n"
    "v_cvt_pk_bf16_f32 v99, v54, v55\n"
    "v_mfma_f32_16x16x32_bf16 v[32:35], a[204:207], a[172:175], v[32:35]\n"
    "v_add_f32 v130, v130, v52\n"
    "v_add_f32 v131, v131, v60\n"
    "v_mfma_f32_16x16x32_bf16 v[40:43], a[204:207], a[188:191], v[40:43]\n"
    "v_cvt_pk_bf16_f32 v100, v56, v57\n"
    "v_add_f32 v130, v130, v53\n"
    "v_mfma_f32_16x16x32_bf16 v[36:39], a[220:223], a[172:175], v[36:39]\n"
    "buffer_load_dwordx4 v127, s[40:43], s66 offen offset:3072 nt lds\n"
    "s_add_u32 s66, s66, 16384\n"
    "v_add_f32 v131, v131, v61\n"
    "v_cvt_pk_bf16_f32 v101, v58, v59\n"
    "v_mfma_f32_16x16x32_bf16 v[44:47], a[220:223], a[188:191], v[44:47]\n"
    "v_add_f32 v130, v130, v54\n"
    "v_add_f32 v131, v131, v62\n"
    "v_mfma_f32_16x16x32_bf16 a[32:35], a[240:243], v[104:107], a[32:35]\n"
    "ds_read_b128 a[192:195], v112 offset:16384\n"
    "v_add_f32 v130, v130, v55\n"
    "v_mfma_f32_16x16x32_bf16 a[48:51], a[240:243], v[108:111], a[48:51]\n"
    "ds_read_b128 a[208:211], v112 offset:20480\n"
    "v_cvt_pk_bf16_f32 v102, v60, v61\n"
    "v_mfma_f32_16x16x32_bf16 a[36:39], a[244:247], v[104:107], a[36:39]\n"
    "ds_read_b128 a[196:199], v113 offset:16384\n"
    "v_add_f32 v131, v131, v63\n"
    "v_mfma_f32_16x16x32_bf16 a[52:55], a[244:247], v[108:111], a[52:55]\n"
    "v_exp_f32 v64, v32\n"
    "v_mfma_f32_16x16x32_bf16 a[40:43], a[248:251], v[104:107], a[40:43]\n"
    "ds_read_b128 a[212:215], v113 offset:20480\n"
    "v_exp_f32 v65, v33\n"
    "v_mfma_f32_16x16x32_bf16 a[56:59], a[248:251], v[108:111], a[56:59]\n"
    "ds_read_b128 a[200:203], v114 offset:16384\n"
    "v_exp_f32 v66, v34\n"
    "v_mfma_f32_16x16x32_bf16 a[44:47], a[252:255], v[104:107], a[44:47]\n"
    "ds_read_b128 a[216:219], v114 offset:20480\n"
    "v_exp_f32 v67, v35\n"
    "v_mfma_f32_16x16x32_bf16 a[60:63], a[252:255], v[108:111], a[60:63]\n"
    "v_exp_f32 v68, v36\n"
    "v_mfma_f32_16x16x32_bf16 v[224:227], a[224:227], v[104:107], v[224:227]\n"
    "ds_read_b128 a[204:207], v115 offset:16384\n"
    "v_max_f32 v15, v130, v131\n"
    "v_mfma_f32_16x16x32_bf16 v[240:243], a[224:227], v[108:111], v[240:243]\n"
    "ds_read_b128 a[220:223], v115 offset:20480\n"
    "v_cvt_pk_bf16_f32 v103, v62, v63\n"
    "v_mfma_f32_16x16x32_bf16 v[228:231], a[228:231], v[104:107], v[228:231]\n"
    "v_exp_f32 v69, v37\n"
    "v_mfma_f32_16x16x32_bf16 v[244:247], a[228:231], v[108:111], v[244:247]\n"
    "v_exp_f32 v70, v38\n"
    "v_mfma_f32_16x16x32_bf16 v[232:235], a[232:235], v[104:107], v[232:235]\n"
    "v_exp_f32 v71, v39\n"
    "v_cmp_nge_f32 vcc, s76, v15\n"
    "v_mfma_f32_16x16x32_bf16 v[248:251], a[232:235], v[108:111], v[248:251]\n"
    "s_memrealtime s[34:35]\n"
    "v_exp_f32 v72, v40\n"
    "v_mfma_f32_16x16x32_bf16 v[236:239], a[236:239], v[104:107], v[236:239]\n"
    "v_exp_f32 v73, v41\n"
    "v_exp_f32 v74, v42\n"
    "v_mfma_f32_16x16x32_bf16 v[252:255], a[236:239], v[108:111], v[252:255]\n"
    "v_exp_f32 v75, v43\n"
    "s_branch .Lattn_loop\n"
    ".Lattn_last:\n"
    "s_cbranch_vccnz .Lrare_a3K\n"
    ".Lback_a3K:\n"
    "s_waitcnt vmcnt(16) lgkmcnt(0)\n"
    "s_sub_u32 s28, s34, s80\n"
    "s_cmp_lt_i32 s28, 0\n"
    "s_cbranch_scc1 .Lwait_3K\n"
    ".Lgo_3K:\n"
    "s_add_u32 s80, s34, s77\n"
    "s_barrier\n"
    "v_mfma_f32_16x16x32_bf16 v[16:19], a[192:195], a[128:131], v[80:83]\n"
    "ds_read_b64_tr_b16 a[240:241], v120 offset:49152\n"
    "v_exp_f32 v76, v44\n"
    "v_mfma_f32_16x16x32_bf16 v[24:27], a[192:195], a[144:147], v[84:87]\n"
    "ds_read_b64_tr_b16 a[242:243], v120 offset:53248\n"
    "v_add_f32 v132, v64, v65\n"
    "v_mfma_f32_16x16x32_bf16 v[20:23], a[208:211], a[128:131], v[80:83]\n"
    "ds_read_b64_tr_b16 a[244:245], v121 offset:49152\n"
    "v_add_f32 v133, v72, v73\n"
    "v_mfma_f32_16x16x32_bf16 v[28:31], a[208:211], a[144:147], v[84:87]\n"
    "ds_read_b64_tr_b16 a[246:247], v121 offset:53248\n"
    "v_add_f32 v132, v132, v66\n"
    "v_mfma_f32_16x16x32_bf16 v[16:19], a[196:199], a[132:135], v[16:19]\n"
    "ds_read_b64_tr_b16 a[248:249], v122 offset:49152\n"
    "v_exp_f32 v77, v45\n"
    "v_mfma_f32_16x16x32_bf16 v[24:27], a[196:199], a[148:151], v[24:27]\n"
    "ds_read_b64_tr_b16 a[250:251], v122 offset:53248\n"
    "v_add_f32 v133, v133, v74\n"
    "v_mfma_f32_16x16x32_bf16 v[20:23], a[212:215], a[132:135], v[20:23]\n"
    "ds_read_b64_tr_b16 a[252:253], v123 offset:49152\n"
    "v_add_f32 v132, v132, v67\n"
    "v_mfma_f32_16x16x32_bf16 v[28:31], a[212:215], a[148:151], v[28:31]\n"
    "ds_read_b64_tr_b16 a[254:255], v123 offset:53248\n"
    "v_add_f32 v133, v133, v75\n"
    "v_mfma_f32_16x16x32_bf16 v[16:19], a[200:203], a[136:139], v[16:19]\n"
    "ds_read_b64_tr_b16 a[224:225], v116 offset:49152\n"
    "v_exp_f32 v78, v46\n"
    "v_mfma_f32_16x16x32_bf16 v[24:27], a[200:203], a[152:155], v[24:27]\n"
    "ds_read_b64_tr_b16 a[226:227], v116 offset:53248\n"
    "v_add_f32 v132, v132, v68\n"
    "v_mfma_f32_16x16x32_bf16 v[20:23], a[216:219], a[136:139], v[20:23]\n"
    "ds_read_b64_tr_b16 a[228:229], v117 offset:49152\n"
    "v_add_f32 v132, v132, v69\n"
    "v_mfma_f32_16x16x32_bf16 v[28:31], a[216:219], a[152:155], v[28:31]\n"
    "ds_read_b64_tr_b16 a[230:231], v117 offset:53248\n"
    "v_cvt_pk_bf16_f32 v104, v64, v65\n"
    "v_mfma_f32_16x16x32_bf16 v[16:19], a[204:207], a[140:143], v[16:19]\n"
    "ds_read_b64_tr_b16 a[232:233], v118 offset:49152\n"
    "v_exp_f32 v79, v47\n"
    "v_mfma_f32_16x16x32_bf16 v[24:27], a[204:207], a[156:159], v[24:27]\n"
    "ds_read_b64_tr_b16 a[234:235], v118 offset:53248\n"
    "v_cvt_pk_bf16_f32 v105, v66, v67\n"
    "v_mfma_f32_16x16x32_bf16 v[20:23], a[220:223], a[140:143], v[20:23]\n"
    "ds_read_b64_tr_b16 a[236:237], v119 offset:49152\n"
    "v_cvt_pk_bf16_f32 v106, v68, v69\n"
    "v_mfma_f32_16x16x32_bf16 v[28:31], a[220:223], a[156:159], v[28:31]\n"
    "ds_read_b64_tr_b16 a[238:239], v119 offset:53248\n"
    "s_waitcnt lgkmcnt(8)\n"
    "v_mfma_f32_16x16x32_bf16 a[0:3], a[240:243], v[96:99], a[0:3]\n"
    "s_add_u32 m0, s71, 40960\n"
    "v_add_f32 v134, v134, v130\n"
    "v_cvt_pk_bf16_f32 v107, v70, v71\n"
    "v_add_f32 v135, v135, v131\n"
    "v_mfma_f32_16x16x32_bf16 a[16:19], a[240:243], v[100:103], a[16:19]\n"
    "buffer_load_dwordx4 v128, s[44:47], s67 offen offset:0 nt lds\n"
    "v_add_f32 v133, v133, v76\n"
    "v_cvt_pk_bf16_f32 v108, v72, v73\n"
    "v_mfma_f32_16x16x32_bf16 a[4:7], a[244:247], v[96:99], a[4:7]\n"
    "buffer_load_dwordx4 v129, s[44:47], s67 offen offset:1024 nt lds\n"
    "v_add_f32 v133, v133, v77\n"
    "v_add_f32 v132, v132, v70\n"
    "v_mfma_f32_16x16x32_bf16 a[20:23], a[244:247], v[100:103], a[20:23]\n"
    "v_exp_f32 v48, v16\n"
    "v_mfma_f32_16x16x32_bf16 a[8:11], a[248:251], v[96:99], a[8:11]\n"
    "v_exp_f32 v49, v17\n"
    "v_mfma_f32_16x16x32_bf16 a[24:27], a[248:251], v[100:103], a[24:27]\n"
    "v_exp_f32 v50, v18\n"
    "v_cvt_pk_bf16_f32 v109, v74, v75\n"
    "v_mfma_f32_16x16x32_bf16 a[12:15], a[252:255], v[96:99], a[12:15]\n"
    "v_exp_f32 v51, v19\n"
    "v_add_f32 v133, v133, v78\n"
    "v_mfma_f32_16x16x32_bf16 a[28:31], a[252:255], v[100:103], a[28:31]\n"
    "s_waitcnt lgkmcnt(0)\n"
    "v_exp_f32 v52, v20\n"
    "v_mfma_f32_16x16x32_bf16 v[192:195], a[224:227], v[96:99], v[192:195]\n"
    "buffer_load_dwordx4 v128, s[44:47], s67 offen offset:2048 nt lds\n"
    "v_exp_f32 v53, v21\n"
    "v_mfma_f32_16x16x32_bf16 v[208:211], a[224:227], v[100:103], v[208:211]\n"
    "v_add_f32 v132, v132, v71\n"
    "v_cvt_pk_bf16_f32 v110, v76, v77\n"
    "v_mfma_f32_16x16x32_bf16 v[196:199], a[228:231], v[96:99], v[196:199]\n"
    "v_exp_f32 v54, v22\n"
    "v_add_f32 v133, v133, v79\n"
    "v_mfma_f32_16x16x32_bf16 v[212:215], a[228:231], v[100:103], v[212:215]\n"
    "v_exp_f32 v55, v23\n"
    "v_mfma_f32_16x16x32_bf16 v[200:203], a[232:235], v[96:99], v[200:203]\n"
    "buffer_load_dwordx4 v129, s[44:47], s67 offen offset:3072 nt lds\n"
    "v_max_f32 v15, v132, v133\n"
    "s_add_u32 s67, s67, 16384\n"
    "v_mfma_f32_16x16x32_bf16 v[216:219], a[232:235], v[100:103], v[216:219]\n"
    "v_exp_f32 v56, v24\n"
    "v_cvt_pk_bf16_f32 v111, v78, v79\n"
    "v_mfma_f32_16x16x32_bf16 v[204:207], a[236:239], v[96:99], v[204:207]\n"
    "v_cmp_nge_f32 vcc, s76, v15\n"
    "v_exp_f32 v57, v25\n"
    "v_mfma_f32_16x16x32_bf16 v[220:223], a[236:239], v[100:103], v[220:223]\n"
    "v_exp_f32 v58, v26\n"
    "s_cbranch_vccnz .Lrare_b3K\n"
    ".Lback_b3K:\n"
    "v_mfma_f32_16x16x32_bf16 v[32:35], a[192:195], a[160:163], v[88:91]\n"
    "s_add_u32 m0, s71, 0\n"
    "v_exp_f32 v59, v27\n"
    "v_mfma_f32_16x16x32_bf16 v[40:43], a[192:195], a[176:179], v[92:95]\n"
    "buffer_load_dwordx4 v144, s[52:55], s68 offen offset:0 lds\n"
    "v_add_f32 v136, v136, v132\n"
    "v_mfma_f32_16x16x32_bf16 v[36:39], a[208:211], a[160:163], v[88:91]\n"
    "v_cvt_pk_bf16_f32 v96, v48, v49\n"
    "v_add_f32 v137, v137, v133\n"
    "v_mfma_f32_16x16x32_bf16 v[44:47], a[208:211], a[176:179], v[92:95]\n"
    "v_exp_f32 v60, v28\n"
    "v_mfma_f32_16x16x32_bf16 v[32:35], a[196:199], a[164:167], v[32:35]\n"
    "v_add_f32 v130, v48, v49\n"
    "v_cvt_pk_bf16_f32 v97, v50, v51\n"
    "v_mfma_f32_16x16x32_bf16 v[40:43], a[196:199], a[180:183], v[40:43]\n"
    "v_exp_f32 v61, v29\n"
    "v_mfma_f32_16x16x32_bf16 v[36:39], a[212:215], a[164:167], v[36:39]\n"
    "buffer_load_dwordx4 v145, s[52:55], s68 offen offset:1024 lds\n"
    "v_add_f32 v131, v56, v57\n"
    "v_mfma_f32_16x16x32_bf16 v[44:47], a[212:215], a[180:183], v[44:47]\n"
    "v_add_f32 v130, v130, v50\n"
    "v_exp_f32 v62, v30\n"
    "v_mfma_f32_16x16x32_bf16 v[32:35], a[200:203], a[168:171], v[32:35]\n"
    "v_cvt_pk_bf16_f32 v98, v52, v53\n"
    "v_add_f32 v131, v131, v58\n"
    "v_mfma_f32_16x16x32_bf16 v[40:43], a[200:203], a[184:187], v[40:43]\n"
    "v_exp_f32 v63, v31\n"
    "v_mfma_f32_16x16x32_bf16 v[36:39], a[216:219], a[168:171], v[36:39]\n"
    "buffer_load_dwordx4 v146, s[52:55], s68 offen offset:2048 lds\n"
    "v_add_f32 v130, v130, v51\n"
    "v_mfma_f32_16x16x32_bf16 v[44:47], a[216:219], a[184:187], v[44:47]\n"
    "v_add_f32 v131, v131, v59\n"
    "v_cvt_pk_bf16_f32 v99, v54, v55\n"
    "v_mfma_f32_16x16x32_bf16 v[32:35], a[204:207], a[172:175], v[32:35]\n"
    "v_add_f32 v130, v130, v52\n"
    "v_add_f32 v131, v131, v60\n"
    "v_mfma_f32_16x16x32_bf16 v[40:43], a[204:207], a[188:191], v[40:43]\n"
    "v_cvt_pk_bf16_f32 v100, v56, v57\n"
    "v_add_f32 v130, v130, v53\n"
    "v_mfma_f32_16x16x32_bf16 v[36:39], a[220:223], a[172:175], v[36:39]\n"
    "buffer_load_dwordx4 v147, s[52:55], s68 offen offset:3072 lds\n"
    "s_add_u32 s68, s68, 131072\n"
    "v_add_f32 v131, v131, v61\n"
    "v_cvt_pk_bf16_f32 v101, v58, v59\n"
    "v_mfma_f32_16x16x32_bf16 v[44:47], a[220:223], a[188:191], v[44:47]\n"
    "v_add_f32 v130, v130, v54\n"
    "v_add_f32 v131, v131, v62\n"
    "v_mfma_f32_16x16x32_bf16 a[32:35], a[240:243], v[104:107], a[32:35]\n"
    "ds_read_b128 a[192:195], v112 offset:16384\n"
    "v_add_f32 v130, v130, v55\n"
    "v_mfma_f32_16x16x32_bf16 a[48:51], a[240:243], v[108:111], a[48:51]\n"
    "ds_read_b128 a[208:211], v112 offset:20480\n"
    "v_cvt_pk_bf16_f32 v102, v60, v61\n"
    "v_mfma_f32_16x16x32_bf16 a[36:39], a[244:247], v[104:107], a[36:39]\n"
    "ds_read_b128 a[196:199], v113 offset:16384\n"
    "v_add_f32 v131, v131, v63\n"
    "v_mfma_f32_16x16x32_bf16 a[52:55], a[244:247], v[108:111], a[52:55]\n"
    "v_exp_f32 v64, v32\n"
    "v_mfma_f32_16x16x32_bf16 a[40:43], a[248:251], v[104:107], a[40:43]\n"
    "ds_read_b128 a[212:215], v113 offset:20480\n"
    "v_exp_f32 v65, v33\n"
    "v_mfma_f32_16x16x32_bf16 a[56:59], a[248:251], v[108:111], a[56:59]\n"
    "ds_read_b128 a[200:203], v114 offset:16384\n"
    "v_exp_f32 v66, v34\n"
    "v_mfma_f32_16x16x32_bf16 a[44:47], a[252:255], v[104:107], a[44:47]\n"
    "ds_read_b128 a[216:219], v114 offset:20480\n"
    "v_exp_f32 v67, v35\n"
    "v_mfma_f32_16x16x32_bf16 a[60:63], a[252:255], v[108:111], a[60:63]\n"
    "v_exp_f32 v68, v36\n"
    "v_mfma_f32_16x16x32_bf16 v[224:227], a[224:227], v[104:107], v[224:227]\n"
    "ds_read_b128 a[204:207], v115 offset:16384\n"
    "v_max_f32 v15, v130, v131\n"
    "v_mfma_f32_16x16x32_bf16 v[240:243], a[224:227], v[108:111], v[240:243]\n"
    "ds_read_b128 a[220:223], v115 offset:20480\n"
    "v_cvt_pk_bf16_f32 v103, v62, v63\n"
    "v_mfma_f32_16x16x32_bf16 v[228:231], a[228:231], v[104:107], v[228:231]\n"
    "v_exp_f32 v69, v37\n"
    "v_mfma_f32_16x16x32_bf16 v[244:247], a[228:231], v[108:111], v[244:247]\n"
    "v_exp_f32 v70, v38\n"
    "v_mfma_f32_16x16x32_bf16 v[232:235], a[232:235], v[104:107], v[232:235]\n"
    "v_exp_f32 v71, v39\n"
    "v_cmp_nge_f32 vcc, s76, v15\n"
    "v_mfma_f32_16x16x32_bf16 v[248:251], a[232:235], v[108:111], v[248:251]\n"
    "s_memrealtime s[34:35]\n"
    "v_exp_f32 v72, v40\n"
    "v_mfma_f32_16x16x32_bf16 v[236:239], a[236:239], v[104:107], v[236:239]\n"
    "v_exp_f32 v73, v41\n"
    "v_exp_f32 v74, v42\n"
    "v_mfma_f32_16x16x32_bf16 v[252:255], a[236:239], v[108:111], v[252:255]\n"
    "v_exp_f32 v75, v43\n"
    "s_cbranch_vccnz .Lrare_a0K\n"
    ".Lback_a0K:\n"
    "s_waitcnt vmcnt(16) lgkmcnt(0)\n"
    "s_sub_u32 s28, s34, s80\n"
    "s_cmp_lt_i32 s28, 0\n"
    "s_cbranch_scc1 .Lwait_0K\n"
    ".Lgo_0K:\n"
    "s_add_u32 s80, s34, s77\n"
    "s_barrier\n"
    "v_mfma_f32_16x16x32_bf16 v[16:19], a[192:195], a[128:131], v[80:83]\n"
    "ds_read_b64_tr_b16 a[240:241], v120 offset:0\n"
    "v_exp_f32 v76, v44\n"
    "v_mfma_f32_16x16x32_bf16 v[24:27], a[192:195], a[144:147], v[84:87]\n"
    "ds_read_b64_tr_b16 a[242:243], v120 offset:4096\n"
    "v_add_f32 v132, v64, v65\n"
    "v_mfma_f32_16x16x32_bf16 v[20:23], a[208:211], a[128:131], v[80:83]\n"
    "ds_read_b64_tr_b16 a[244:245], v121 offset:0\n"
    "v_add_f32 v133, v72, v73\n"
    "v_mfma_f32_16x16x32_bf16 v[28:31], a[208:211], a[144:147], v[84:87]\n"
    "ds_read_b64_tr_b16 a[246:247], v121 offset:4096\n"
    "v_add_f32 v132, v132, v66\n"
    "v_mfma_f32_16x16x32_bf16 v[16:19], a[196:199], a[132:135], v[16:19]\n"
    "ds_read_b64_tr_b16 a[248:249], v122 offset:0\n"
    "v_exp_f32 v77, v45\n"
    "v_mfma_f32_16x16x32_bf16 v[24:27], a[196:199], a[148:151], v[24:27]\n"
    "ds_read_b64_tr_b16 a[250:251], v122 offset:4096\n"
    "v_add_f32 v133, v133, v74\n"
    "v_mfma_f32_16x16x32_bf16 v[20:23], a[212:215], a[132:135], v[20:23]\n"
    "ds_read_b64_tr_b16 a[252:253], v123 offset:0\n"
    "v_add_f32 v132, v132, v67\n"
    "v_mfma_f32_16x16x32_bf16 v[28:31], a[212:215], a[148:151], v[28:31]\n"
    "ds_read_b64_tr_b16 a[254:255], v123 offset:4096\n"
    "v_add_f32 v133, v133, v75\n"
    "v_mfma_f32_16x16x32_bf16 v[16:19], a[200:203], a[136:139], v[16:19]\n"
    "ds_read_b64_tr_b16 a[224:225], v116 offset:0\n"
    "v_exp_f32 v78, v46\n"
    "v_mfma_f32_16x16x32_bf16 v[24:27], a[200:203], a[152:155], v[24:27]\n"
    "ds_read_b64_tr_b16 a[226:227], v116 offset:4096\n"
    "v_add_f32 v132, v132, v68\n"
    "v_mfma_f32_16x16x32_bf16 v[20:23], a[216:219], a[136:139], v[20:23]\n"
    "ds_read_b64_tr_b16 a[228:229], v117 offset:0\n"
    "v_add_f32 v132, v132, v69\n"
    "v_mfma_f32_16x16x32_bf16 v[28:31], a[216:219], a[152:155], v[28:31]\n"
    "ds_read_b64_tr_b16 a[230:231], v117 offset:4096\n"
    "v_cvt_pk_bf16_f32 v104, v64, v65\n"
    "v_mfma_f32_16x16x32_bf16 v[16:19], a[204:207], a[140:143], v[16:19]\n"
    "ds_read_b64_tr_b16 a[232:233], v118 offset:0\n"
    "v_exp_f32 v79, v47\n"
    "v_mfma_f32_16x16x32_bf16 v[24:27], a[204:207], a[156:159], v[24:27]\n"
    "ds_read_b64_tr_b16 a[234:235], v118 offset:4096\n"
    "v_cvt_pk_bf16_f32 v105, v66, v67\n"
    "v_mfma_f32_16x16x32_bf16 v[20:23], a[220:223], a[140:143], v[20:23]\n"
    "ds_read_b64_tr_b16 a[236:237], v119 offset:0\n"
    "v_cvt_pk_bf16_f32 v106, v68, v69\n"
    "v_mfma_f32_16x16x32_bf16 v[28:31], a[220:223], a[156:159], v[28:31]\n"
    "ds_read_b64_tr_b16 a[238:239], v119 offset:4096\n"
    "s_waitcnt lgkmcnt(8)\n"
    "v_mfma_f32_16x16x32_bf16 a[0:3], a[240:243], v[96:99], a[0:3]\n"
    "s_add_u32 m0, s71, 57344\n"
    "v_add_f32 v134, v134, v130\n"
    "v_cvt_pk_bf16_f32 v107, v70, v71\n"
    "v_add_f32 v135, v135, v131\n"
    "v_mfma_f32_16x16x32_bf16 a[16:19], a[240:243], v[100:103], a[16:19]\n"
    "buffer_load_dwordx4 v128, s[44:47], s67 offen offset:0 nt lds\n"
    "v_add_f32 v133, v133, v76\n"
    "v_cvt_pk_bf16_f32 v108, v72, v73\n"
    "v_mfma_f32_16x16x32_bf16 a[4:7], a[244:247], v[96:99], a[4:7]\n"
    "buffer_load_dwordx4 v129, s[44:47], s67 offen offset:1024 nt lds\n"
    "v_add_f32 v133, v133, v77\n"
    "v_add_f32 v132, v132, v70\n"
    "v_mfma_f32_16x16x32_bf16 a[20:23], a[244:247], v[100:103], a[20:23]\n"
    "v_exp_f32 v48, v16\n"
    "v_mfma_f32_16x16x32_bf16 a[8:11], a[248:251], v[96:99], a[8:11]\n"
    "v_exp_f32 v49, v17\n"
    "v_mfma_f32_16x16x32_bf16 a[24:27], a[248:251], v[100:103], a[24:27]\n"
    "v_exp_f32 v50, v18\n"
    "v_cvt_pk_bf16_f32 v109, v74, v75\n"
    "v_mfma_f32_16x16x32_bf16 a[12:15], a[252:255], v[96:99], a[12:15]\n"
    "v_exp_f32 v51, v19\n"
    "v_add_f32 v133, v133, v78\n"
    "v_mfma_f32_16x16x32_bf16 a[28:31], a[252:255], v[100:103], a[28:31]\n"
    "s_waitcnt lgkmcnt(0)\n"
    "v_exp_f32 v52, v20\n"
    "v_mfma_f32_16x16x32_bf16 v[192:195], a[224:227], v[96:99], v[192:195]\n"
    "buffer_load_dwordx4 v128, s[44:47], s67 offen offset:2048 nt lds\n"
    "v_exp_f32 v53, v21\n"
    "v_mfma_f32_16x16x32_bf16 v[208:211], a[224:227], v[100:103], v[208:211]\n"
    "v_add_f32 v132, v132, v71\n"
    "v_cvt_pk_bf16_f32 v110, v76, v77\n"
    "v_mfma_f32_16x16x32_bf16 v[196:199], a[228:231], v[96:99], v[196:199]\n"
    "v_exp_f32 v54, v22\n"
    "v_add_f32 v133, v133, v79\n"
    "v_mfma_f32_16x16x32_bf16 v[212:215], a[228:231], v[100:103], v[212:215]\n"
    "v_exp_f32 v55, v23\n"
    "v_mfma_f32_16x16x32_bf16 v[200:203], a[232:235], v[96:99], v[200:203]\n"
    "buffer_load_dwordx4 v129, s[44:47], s67 offen offset:3072 nt lds\n"
    "v_max_f32 v15, v132, v133\n"
    "s_add_u32 s67, s67, 16384\n"
    "v_mfma_f32_16x16x32_bf16 v[216:219], a[232:235], v[100:103], v[216:219]\n"
    "v_exp_f32 v56, v24\n"
    "v_cvt_pk_bf16_f32 v111, v78, v79\n"
    "v_mfma_f32_16x16x32_bf16 v[204:207], a[236:239], v[96:99], v[204:207]\n"
    "v_cmp_nge_f32 vcc, s76, v15\n"
    "v_exp_f32 v57, v25\n"
    "v_mfma_f32_16x16x32_bf16 v[220:223], a[236:239], v[100:103], v[220:223]\n"
    "v_exp_f32 v58, v26\n"
    "s_cbranch_vccnz .Lrare_b0K\n"
    ".Lback_b0K:\n"
    "v_mfma_f32_16x16x32_bf16 v[32:35], a[192:195], a[160:163], v[88:91]\n"
    "s_add_u32 m0, s71, 16384\n"
    "v_exp_f32 v59, v27\n"
    "v_mfma_f32_16x16x32_bf16 v[40:43], a[192:195], a[176:179], v[92:95]\n"
    "buffer_load_dwordx4 v144, s[52:55], s68 offen offset:0 lds\n"
    "v_add_f32 v136, v136, v132\n"
    "v_mfma_f32_16x16x32_bf16 v[36:39], a[208:211], a[160:163], v[88:91]\n"
    "v_cvt_pk_bf16_f32 v96, v48, v49\n"
    "v_add_f32 v137, v137, v133\n"
    "v_mfma_f32_16x16x32_bf16 v[44:47], a[208:211], a[176:179], v[92:95]\n"
    "v_exp_f32 v60, v28\n"
    "v_mfma_f32_16x16x32_bf16 v[32:35], a[196:199], a[164:167], v[32:35]\n"
    "v_add_f32 v130, v48, v49\n"
    "v_cvt_pk_bf16_f32 v97, v50, v51\n"
    "v_mfma_f32_16x16x32_bf16 v[40:43], a[196:199], a[180:183], v[40:43]\n"
    "v_exp_f32 v61, v29\n"
    "v_mfma_f32_16x16x32_bf16 v[36:39], a[212:215], a[164:167], v[36:39]\n"
    "buffer_load_dwordx4 v145, s[52:55], s68 offen offset:1024 lds\n"
    "v_add_f32 v131, v56, v57\n"
    "v_mfma_f32_16x16x32_bf16 v[44:47], a[212:215], a[180:183], v[44:47]\n"
    "v_add_f32 v130, v130, v50\n"
    "v_exp_f32 v62, v30\n"
    "v_mfma_f32_16x16x32_bf16 v[32:35], a[200:203], a[168:171], v[32:35]\n"
    "v_cvt_pk_bf16_f32 v98, v52, v53\n"
    "v_add_f32 v131, v131, v58\n"
    "v_mfma_f32_16x16x32_bf16 v[40:43], a[200:203], a[184:187], v[40:43]\n"
    "v_exp_f32 v63, v31\n"
    "v_mfma_f32_16x16x32_bf16 v[36:39], a[216:219], a[168:171], v[36:39]\n"
    "buffer_load_dwordx4 v146, s[52:55], s68 offen offset:2048 lds\n"
    "v_add_f32 v130, v130, v51\n"
    "v_mfma_f32_16x16x32_bf16 v[44:47], a[216:219], a[184:187], v[44:47]\n"
    "v_add_f32 v131, v131, v59\n"
    "v_cvt_pk_bf16_f32 v99, v54, v55\n"
    "v_mfma_f32_16x16x32_bf16 v[32:35], a[204:207], a[172:175], v[32:35]\n"
    "v_add_f32 v130, v130, v52\n"
    "v_add_f32 v131, v131, v60\n"
    "v_mfma_f32_16x16x32_bf16 v[40:43], a[204:207], a[188:191], v[40:43]\n"
    "v_cvt_pk_bf16_f32 v100, v56, v57\n"
    "v_add_f32 v130, v130, v53\n"
    "v_mfma_f32_16x16x32_bf16 v[36:39], a[220:223], a[172:175], v[36:39]\n"
    "buffer_load_dwordx4 v147, s[52:55], s68 offen offset:3072 lds\n"
    "s_add_u32 s68, s68, 131072\n"
    "v_add_f32 v131, v131, v61\n"
    "v_cvt_pk_bf16_f32 v101, v58, v59\n"
    "v_mfma_f32_16x16x32_bf16 v[44:47], a[220:223], a[188:191], v[44:47]\n"
    "v_add_f32 v130, v130, v54\n"
    "v_add_f32 v131, v131, v62\n"
    "v_mfma_f32_16x16x32_bf16 a[32:35], a[240:243], v[104:107], a[32:35]\n"
    "ds_read_b128 a[192:195], v112 offset:32768\n"
    "v_add_f32 v130, v130, v55\n"
    "v_mfma_f32_16x16x32_bf16 a[48:51], a[240:243], v[108:111], a[48:51]\n"
    "ds_read_b128 a[208:211], v112 offset:36864\n"
    "v_cvt_pk_bf16_f32 v102, v60, v61\n"
    "v_mfma_f32_16x16x32_bf16 a[36:39], a[244:247], v[104:107], a[36:39]\n"
    "ds_read_b128 a[196:199], v113 offset:32768\n"
    "v_add_f32 v131, v131, v63\n"
    "v_mfma_f32_16x16x32_bf16 a[52:55], a[244:247], v[108:111], a[52:55]\n"
    "v_exp_f32 v64, v32\n"
    "v_mfma_f32_16x16x32_bf16 a[40:43], a[248:251], v[104:107], a[40:43]\n"
    "ds_read_b128 a[212:215], v113 offset:36864\n"
    "v_exp_f32 v65, v33\n"
    "v_mfma_f32_16x16x32_bf16 a[56:59], a[248:251], v[108:111], a[56:59]\n"
    "ds_read_b128 a[200:203], v114 offset:32768\n"
    "v_exp_f32 v66, v34\n"
    "v_mfma_f32_16x16x32_bf16 a[44:47], a[252:255], v[104:107], a[44:47]\n"
    "ds_read_b128 a[216:219], v114 offset:36864\n"
    "v_exp_f32 v67, v35\n"
    "v_mfma_f32_16x16x32_bf16 a[60:63], a[252:255], v[108:111], a[60:63]\n"
    "v_exp_f32 v68, v36\n"
    "v_mfma_f32_16x16x32_bf16 v[224:227], a[224:227], v[104:107], v[224:227]\n"
    "ds_read_b128 a[204:207], v115 offset:32768\n"
    "v_max_f32 v15, v130, v131\n"
    "v_mfma_f32_16x16x32_bf16 v[240:243], a[224:227], v[108:111], v[240:243]\n"
    "ds_read_b128 a[220:223], v115 offset:36864\n"
    "v_cvt_pk_bf16_f32 v103, v62, v63\n"
    "v_mfma_f32_16x16x32_bf16 v[228:231], a[228:231], v[104:107], v[228:231]\n"
    "v_exp_f32 v69, v37\n"
    "v_mfma_f32_16x16x32_bf16 v[244:247], a[228:231], v[108:111], v[244:247]\n"
    "v_exp_f32 v70, v38\n"
    "v_mfma_f32_16x16x32_bf16 v[232:235], a[232:235], v[104:107], v[232:235]\n"
    "v_exp_f32 v71, v39\n"
    "v_cmp_nge_f32 vcc, s76, v15\n"
    "v_mfma_f32_16x16x32_bf16 v[248:251], a[232:235], v[108:111], v[248:251]\n"
    "s_memrealtime s[34:35]\n"
    "v_exp_f32 v72, v40\n"
    "v_mfma_f32_16x16x32_bf16 v[236:239], a[236:239], v[104:107], v[236:239]\n"
    "v_exp_f32 v73, v41\n"
    "v_exp_f32 v74, v42\n"
    "v_mfma_f32_16x16x32_bf16 v[252:255], a[236:239], v[108:111], v[252:255]\n"
    "v_exp_f32 v75, v43\n"
    "s_cbranch_vccnz .Lrare_a1KV\n"
    ".Lback_a1KV:\n"
    "s_waitcnt vmcnt(16) lgkmcnt(0)\n"
    "s_sub_u32 s28, s34, s80\n"
    "s_cmp_lt_i32 s28, 0\n"
    "s_cbranch_scc1 .Lwait_1KV\n"
    ".Lgo_1KV:\n"
    "s_add_u32 s80, s34, s77\n"
    "s_barrier\n"
    "v_mfma_f32_16x16x32_bf16 v[16:19], a[192:195], a[128:131], v[80:83]\n"
    "ds_read_b64_tr_b16 a[240:241], v120 offset:16384\n"
    "v_exp_f32 v76, v44\n"
    "v_mfma_f32_16x16x32_bf16 v[24:27], a[192:195], a[144:147], v[84:87]\n"
    "ds_read_b64_tr_b16 a[242:243], v120 offset:20480\n"
    "v_add_f32 v132, v64, v65\n"
    "v_mfma_f32_16x16x32_bf16 v[20:23], a[208:211], a[128:131], v[80:83]\n"
    "ds_read_b64_tr_b16 a[244:245], v121 offset:16384\n"
    "v_add_f32 v133, v72, v73\n"
    "v_mfma_f32_16x16x32_bf16 v[28:31], a[208:211], a[144:147], v[84:87]\n"
    "ds_read_b64_tr_b16 a[246:247], v121 offset:20480\n"
    "v_add_f32 v132, v132, v66\n"
    "v_mfma_f32_16x16x32_bf16 v[16:19], a[196:199], a[132:135], v[16:19]\n"
    "ds_read_b64_tr_b16 a[248:249], v122 offset:16384\n"
    "v_exp_f32 v77, v45\n"
    "v_mfma_f32_16x16x32_bf16 v[24:27], a[196:199], a[148:151], v[24:27]\n"
    "ds_read_b64_tr_b16 a[250:251], v122 offset:20480\n"
    "v_add_f32 v133, v133, v74\n"
    "v_mfma_f32_16x16x32_bf16 v[20:23], a[212:215], a[132:135], v[20:23]\n"
    "ds_read_b64_tr_b16 a[252:253], v123 offset:16384\n"
    "v_add_f32 v132, v132, v67\n"
    "v_mfma_f32_16x16x32_bf16 v[28:31], a[212:215], a[148:151], v[28:31]\n"
    "ds_read_b64_tr_b16 a[254:255], v123 offset:20480\n"
    "v_add_f32 v133, v133, v75\n"
    "v_mfma_f32_16x16x32_bf16 v[16:19], a[200:203], a[136:139], v[16:19]\n"
    "ds_read_b64_tr_b16 a[224:225], v116 offset:16384\n"
    "v_exp_f32 v78, v46\n"
    "v_mfma_f32_16x16x32_bf16 v[24:27], a[200:203], a[152:155], v[24:27]\n"
    "ds_read_b64_tr_b16 a[226:227], v116 offset:20480\n"
    "v_add_f32 v132, v132, v68\n"
    "v_mfma_f32_16x16x32_bf16 v[20:23], a[216:219], a[136:139], v[20:23]\n"
    "ds_read_b64_tr_b16 a[228:229], v117 offset:16384\n"
    "v_add_f32 v132, v132, v69\n"
    "v_mfma_f32_16x16x32_bf16 v[28:31], a[216:219], a[152:155], v[28:31]\n"
    "ds_read_b64_tr_b16 a[230:231], v117 offset:20480\n"
    "v_cvt_pk_bf16_f32 v104, v64, v65\n"
    "v_mfma_f32_16x16x32_bf16 v[16:19], a[204:207], a[140:143], v[16:19]\n"
    "ds_read_b64_tr_b16 a[232:233], v118 offset:16384\n"
    "v_exp_f32 v79, v47\n"
    "v_mfma_f32_16x16x32_bf16 v[24:27], a[204:207], a[156:159], v[24:27]\n"
    "ds_read_b64_tr_b16 a[234:235], v118 offset:20480\n"
    "v_cvt_pk_bf16_f32 v105, v66, v67\n"
    "v_mfma_f32_16x16x32_bf16 v[20:23], a[220:223], a[140:143], v[20:23]\n"
    "ds_read_b64_tr_b16 a[236:237], v119 offset:16384\n"
    "v_cvt_pk_bf16_f32 v106, v68, v69\n"
    "v_mfma_f32_16x16x32_bf16 v[28:31], a[220:223], a[156:159], v[28:31]\n"
    "ds_read_b64_tr_b16 a[238:239], v119 offset:20480\n"
    "s_waitcnt lgkmcnt(8)\n"
    "v_mfma_f32_16x16x32_bf16 a[0:3], a[240:243], v[96:99], a[0:3]\n"
    "s_add_u32 m0, s71, 8192\n"
    "v_add_f32 v134, v134, v130\n"
    "v_cvt_pk_bf16_f32 v107, v70, v71\n"
    "v_add_f32 v135, v135, v131\n"
    "v_mfma_f32_16x16x32_bf16 a[16:19], a[240:243], v[100:103], a[16:19]\n"
    "buffer_load_dwordx4 v144, s[52:55], s69 offen offset:0 lds\n"
    "v_add_f32 v133, v133, v76\n"
    "v_cvt_pk_bf16_f32 v108, v72, v73\n"
    "v_mfma_f32_16x16x32_bf16 a[4:7], a[244:247], v[96:99], a[4:7]\n"
    "buffer_load_dwordx4 v145, s[52:55], s69 offen offset:1024 lds\n"
    "v_add_f32 v133, v133, v77\n"
    "v_add_f32 v132, v132, v70\n"
    "v_mfma_f32_16x16x32_bf16 a[20:23], a[244:247], v[100:103], a[20:23]\n"
    "v_exp_f32 v48, v16\n"
    "v_mfma_f32_16x16x32_bf16 a[8:11], a[248:251], v[96:99], a[8:11]\n"
    "v_exp_f32 v49, v17\n"
    "v_mfma_f32_16x16x32_bf16 a[24:27], a[248:251], v[100:103], a[24:27]\n"
    "v_exp_f32 v50, v18\n"
    "v_cvt_pk_bf16_f32 v109, v74, v75\n"
    "v_mfma_f32_16x16x32_bf16 a[12:15], a[252:255], v[96:99], a[12:15]\n"
    "v_exp_f32 v51, v19\n"
    "v_add_f32 v133, v133, v78\n"
    "v_mfma_f32_16x16x32_bf16 a[28:31], a[252:255], v[100:103], a[28:31]\n"
    "s_waitcnt lgkmcnt(0)\n"
    "v_exp_f32 v52, v20\n"
    "v_mfma_f32_16x16x32_bf16 v[192:195], a[224:227], v[96:99], v[192:195]\n"
    "buffer_load_dwordx4 v146, s[52:55], s69 offen offset:2048 lds\n"
    "v_exp_f32 v53, v21\n"
    "v_mfma_f32_16x16x32_bf16 v[208:211], a[224:227], v[100:103], v[208:211]\n"
    "v_add_f32 v132, v132, v71\n"
    "v_cvt_pk_bf16_f32 v110, v76, v77\n"
    "v_mfma_f32_16x16x32_bf16 v[196:199], a[228:231], v[96:99], v[196:199]\n"
    "v_exp_f32 v54, v22\n"
    "v_add_f32 v133, v133, v79\n"
    "v_mfma_f32_16x16x32_bf16 v[212:215], a[228:231], v[100:103], v[212:215]\n"
    "v_exp_f32 v55, v23\n"
    "v_mfma_f32_16x16x32_bf16 v[200:203], a[232:235], v[96:99], v[200:203]\n"
    "buffer_load_dwordx4 v147, s[52:55], s69 offen offset:3072 lds\n"
    "v_max_f32 v15, v132, v133\n"
    "s_add_u32 s69, s69, 131072\n"
    "v_mfma_f32_16x16x32_bf16 v[216:219], a[232:235], v[100:103], v[216:219]\n"
    "v_exp_f32 v56, v24\n"
    "v_cvt_pk_bf16_f32 v111, v78, v79\n"
    "v_mfma_f32_16x16x32_bf16 v[204:207], a[236:239], v[96:99], v[204:207]\n"
    "v_cmp_nge_f32 vcc, s76, v15\n"
    "v_exp_f32 v57, v25\n"
    "v_mfma_f32_16x16x32_bf16 v[220:223], a[236:239], v[100:103], v[220:223]\n"
    "v_exp_f32 v58, v26\n"
    "s_cbranch_vccnz .Lrare_b1KV\n"
    ".Lback_b1KV:\n"
    "v_mfma_f32_16x16x32_bf16 v[32:35], a[192:195], a[160:163], v[88:91]\n"
    "s_add_u32 m0, s71, 32768\n"
    "v_exp_f32 v59, v27\n"
    "v_mfma_f32_16x16x32_bf16 v[40:43], a[192:195], a[176:179], v[92:95]\n"
    "buffer_load_dwordx4 v144, s[52:55], s68 offen offset:0 lds\n"
    "v_add_f32 v136, v136, v132\n"
    "v_mfma_f32_16x16x32_bf16 v[36:39], a[208:211], a[160:163], v[88:91]\n"
    "v_cvt_pk_bf16_f32 v96, v48, v49\n"
    "v_add_f32 v137, v137, v133\n"
    "v_mfma_f32_16x16x32_bf16 v[44:47], a[208:211], a[176:179], v[92:95]\n"
    "v_exp_f32 v60, v28\n"
    "v_mfma_f32_16x16x32_bf16 v[32:35], a[196:199], a[164:167], v[32:35]\n"
    "v_add_f32 v130, v48, v49\n"
    "v_cvt_pk_bf16_f32 v97, v50, v51\n"
    "v_mfma_f32_16x16x32_bf16 v[40:43], a[196:199], a[180:183], v[40:43]\n"
    "v_exp_f32 v61, v29\n"
    "v_mfma_f32_16x16x32_bf16 v[36:39], a[212:215], a[164:167], v[36:39]\n"
    "buffer_load_dwordx4 v145, s[52:55], s68 offen offset:1024 lds\n"
    "v_add_f32 v131, v56, v57\n"
    "v_mfma_f32_16x16x32_bf16 v[44:47], a[212:215], a[180:183], v[44:47]\n"
    "v_add_f32 v130, v130, v50\n"
    "v_exp_f32 v62, v30\n"
    "v_mfma_f32_16x16x32_bf16 v[32:35], a[200:203], a[168:171], v[32:35]\n"
    "v_cvt_pk_bf16_f32 v98, v52, v53\n"
    "v_add_f32 v131, v131, v58\n"
    "v_mfma_f32_16x16x32_bf16 v[40:43], a[200:203], a[184:187], v[40:43]\n"
    "v_exp_f32 v63, v31\n"
    "v_mfma_f32_16x16x32_bf16 v[36:39], a[216:219], a[168:171], v[36:39]\n"
    "buffer_load_dwordx4 v146, s[52:55], s68 offen offset:2048 lds\n"
    "v_add_f32 v130, v130, v51\n"
    "v_mfma_f32_16x16x32_bf16 v[44:47], a[216:219], a[184:187], v[44:47]\n"
    "v_add_f32 v131, v131, v59\n"
    "v_cvt_pk_bf16_f32 v99, v54, v55\n"
    "v_mfma_f32_16x16x32_bf16 v[32:35], a[204:207], a[172:175], v[32:35]\n"
    "v_add_f32 v130, v130, v52\n"
    "v_add_f32 v131, v131, v60\n"
    "v_mfma_f32_16x16x32_bf16 v[40:43], a[204:207], a[188:191], v[40:43]\n"
    "v_cvt_pk_bf16_f32 v100, v56, v57\n"
    "v_add_f32 v130, v130, v53\n"
    "v_mfma_f32_16x16x32_bf16 v[36:39], a[220:223], a[172:175], v[36:39]\n"
    "buffer_load_dwordx4 v147, s[52:55], s68 offen offset:3072 lds\n"
    "s_add_u32 s68, s68, 131072\n"
    "v_add_f32 v131, v131, v61\n"
    "v_cvt_pk_bf16_f32 v101, v58, v59\n"
    "v_mfma_f32_16x16x32_bf16 v[44:47], a[220:223], a[188:191], v[44:47]\n"
    "v_add_f32 v130, v130, v54\n"
    "v_add_f32 v131, v131, v62\n"
    "v_mfma_f32_16x16x32_bf16 a[32:35], a[240:243], v[104:107], a[32:35]\n"
    "ds_read_b128 a[192:195], v112 offset:49152\n"
    "v_add_f32 v130, v130, v55\n"
    "v_mfma_f32_16x16x32_bf16 a[48:51], a[240:243], v[108:111], a[48:51]\n"
    "ds_read_b128 a[208:211], v112 offset:53248\n"
    "v_cvt_pk_bf16_f32 v102, v60, v61\n"
    "v_mfma_f32_16x16x32_bf16 a[36:39], a[244:247], v[104:107], a[36:39]\n"
    "ds_read_b128 a[196:199], v113 offset:49152\n"
    "v_add_f32 v131, v131, v63\n"
    "v_mfma_f32_16x16x32_bf16 a[52:55], a[244:247], v[108:111], a[52:55]\n"
    "v_exp_f32 v64, v32\n"
    "v_mfma_f32_16x16x32_bf16 a[40:43], a[248:251], v[104:107], a[40:43]\n"
    "ds_read_b128 a[212:215], v113 offset:53248\n"
    "v_exp_f32 v65, v33\n"
    "v_mfma_f32_16x16x32_bf16 a[56:59], a[248:251], v[108:111], a[56:59]\n"
    "ds_read_b128 a[200:203], v114 offset:49152\n"
    "v_exp_f32 v66, v34\n"
    "v_mfma_f32_16x16x32_bf16 a[44:47], a[252:255], v[104:107], a[44:47]\n"
    "ds_read_b128 a[216:219], v114 offset:53248\n"
    "v_exp_f32 v67, v35\n"
    "v_mfma_f32_16x16x32_bf16 a[60:63], a[252:255], v[108:111], a[60:63]\n"
    "v_exp_f32 v68, v36\n"
    "v_mfma_f32_16x16x32_bf16 v[224:227], a[224:227], v[104:107], v[224:227]\n"
    "ds_read_b128 a[204:207], v115 offset:49152\n"
    "v_max_f32 v15, v130, v131\n"
    "v_mfma_f32_16x16x32_bf16 v[240:243], a[224:227], v[108:111], v[240:243]\n"
    "ds_read_b128 a[220:223], v115 offset:53248\n"
    "v_cvt_pk_bf16_f32 v103, v62, v63\n"
    "v_mfma_f32_16x16x32_bf16 v[228:231], a[228:231], v[104:107], v[228:231]\n"
    "v_exp_f32 v69, v37\n"
    "v_mfma_f32_16x16x32_bf16 v[244:247], a[228:231], v[108:111], v[244:247]\n"
    "v_exp_f32 v70, v38\n"
    "v_mfma_f32_16x16x32_bf16 v[232:235], a[232:235], v[104:107], v[232:235]\n"
    "v_exp_f32 v71, v39\n"
    "v_cmp_nge_f32 vcc, s76, v15\n"
    "v_mfma_f32_16x16x32_bf16 v[248:251], a[232:235], v[108:111], v[248:251]\n"
    "s_memrealtime s[34:35]\n"
    "v_exp_f32 v72, v40\n"
    "v_mfma_f32_16x16x32_bf16 v[236:239], a[236:239], v[104:107], v[236:239]\n"
    "v_exp_f32 v73, v41\n"
    "v_exp_f32 v74, v42\n"
    "v_mfma_f32_16x16x32_bf16 v[252:255], a[236:239], v[108:111], v[252:255]\n"
    "v_exp_f32 v75, v43\n"
    "buffer_load_dword v156, v12, s[48:51], s13 offen sc1\n"
    "s_cbranch_vccnz .Lrare_a2KV\n"
    ".Lback_a2KV:\n"
    "s_waitcnt vmcnt(16) lgkmcnt(0)\n"
    "s_sub_u32 s28, s34, s80\n"
    "s_cmp_lt_i32 s28, 0\n"
    "s_cbranch_scc1 .Lwait_2KV\n"
    ".Lgo_2KV:\n"
    "s_add_u32 s80, s34, s77\n"
    "s_barrier\n"
    "v_mfma_f32_16x16x32_bf16 v[16:19], a[192:195], a[128:131], v[80:83]\n"
    "ds_read_b64_tr_b16 a[240:241], v120 offset:32768\n"
    "v_exp_f32 v76, v44\n"
    "v_mfma_f32_16x16x32_bf16 v[24:27], a[192:195], a[144:147], v[84:87]\n"
    "ds_read_b64_tr_b16 a[242:243], v120 offset:36864\n"
    "v_add_f32 v132, v64, v65\n"
    "v_mfma_f32_16x16x32_bf16 v[20:23], a[208:211], a[128:131], v[80:83]\n"
    "ds_read_b64_tr_b16 a[244:245], v121 offset:32768\n"
    "v_add_f32 v133, v72, v73\n"
    "v_mfma_f32_16x16x32_bf16 v[28:31], a[208:211], a[144:147], v[84:87]\n"
    "ds_read_b64_tr_b16 a[246:247], v121 offset:36864\n"
    "v_add_f32 v132, v132, v66\n"
    "v_mfma_f32_16x16x32_bf16 v[16:19], a[196:199], a[132:135], v[16:19]\n"
    "ds_read_b64_tr_b16 a[248:249], v122 offset:32768\n"
    "v_exp_f32 v77, v45\n"
    "v_mfma_f32_16x16x32_bf16 v[24:27], a[196:199], a[148:151], v[24:27]\n"
    "ds_read_b64_tr_b16 a[250:251], v122 offset:36864\n"
    "v_add_f32 v133, v133, v74\n"
    "v_mfma_f32_16x16x32_bf16 v[20:23], a[212:215], a[132:135], v[20:23]\n"
    "ds_read_b64_tr_b16 a[252:253], v123 offset:32768\n"
    "v_add_f32 v132, v132, v67\n"
    "v_mfma_f32_16x16x32_bf16 v[28:31], a[212:215], a[148:151], v[28:31]\n"
    "ds_read_b64_tr_b16 a[254:255], v123 offset:36864\n"
    "v_add_f32 v133, v133, v75\n"
    "v_mfma_f32_16x16x32_bf16 v[16:19], a[200:203], a[136:139], v[16:19]\n"
    "ds_read_b64_tr_b16 a[224:225], v116 offset:32768\n"
    "v_exp_f32 v78, v46\n"
    "v_mfma_f32_16x16x32_bf16 v[24:27], a[200:203], a[152:155], v[24:27]\n"
    "ds_read_b64_tr_b16 a[226:227], v116 offset:36864\n"
    "v_add_f32 v132, v132, v68\n"
    "v_mfma_f32_16x16x32_bf16 v[20:23], a[216:219], a[136:139], v[20:23]\n"
    "ds_read_b64_tr_b16 a[228:229], v117 offset:32768\n"
    "v_add_f32 v132, v132, v69\n"
    "v_mfma_f32_16x16x32_bf16 v[28:31], a[216:219], a[152:155], v[28:31]\n"
    "ds_read_b64_tr_b16 a[230:231], v117 offset:36864\n"
    "v_cvt_pk_bf16_f32 v104, v64, v65\n"
    "v_mfma_f32_16x16x32_bf16 v[16:19], a[204:207], a[140:143], v[16:19]\n"
    "ds_read_b64_tr_b16 a[232:233], v118 offset:32768\n"
    "v_exp_f32 v79, v47\n"
    "v_mfma_f32_16x16x32_bf16 v[24:27], a[204:207], a[156:159], v[24:27]\n"
    "ds_read_b64_tr_b16 a[234:235], v118 offset:36864\n"
    "v_cvt_pk_bf16_f32 v105, v66, v67\n"
    "v_mfma_f32_16x16x32_bf16 v[20:23], a[220:223], a[140:143], v[20:23]\n"
    "ds_read_b64_tr_b16 a[236:237], v119 offset:32768\n"
    "v_cvt_pk_bf16_f32 v106, v68, v69\n"
    "v_mfma_f32_16x16x32_bf16 v[28:31], a[220:223], a[156:159], v[28:31]\n"
    "ds_read_b64_tr_b16 a[238:239], v119 offset:36864\n"
    "s_waitcnt lgkmcnt(8)\n"
    "v_mfma_f32_16x16x32_bf16 a[0:3], a[240:243], v[96:99], a[0:3]\n"
    "s_add_u32 m0, s71, 24576\n"
    "v_add_f32 v134, v134, v130\n"
    "v_cvt_pk_bf16_f32 v107, v70, v71\n"
    "v_add_f32 v135, v135, v131\n"
    "v_mfma_f32_16x16x32_bf16 a[16:19], a[240:243], v[100:103], a[16:19]\n"
    "buffer_load_dwordx4 v144, s[52:55], s69 offen offset:0 lds\n"
    "v_add_f32 v133, v133, v76\n"
    "v_cvt_pk_bf16_f32 v108, v72, v73\n"
    "v_mfma_f32_16x16x32_bf16 a[4:7], a[244:247], v[96:99], a[4:7]\n"
    "buffer_load_dwordx4 v145, s[52:55], s69 offen offset:1024 lds\n"
    "v_add_f32 v133, v133, v77\n"
    "v_add_f32 v132, v132, v70\n"
    "v_mfma_f32_16x16x32_bf16 a[20:23], a[244:247], v[100:103], a[20:23]\n"
    "v_exp_f32 v48, v16\n"
    "v_mfma_f32_16x16x32_bf16 a[8:11], a[248:251], v[96:99], a[8:11]\n"
    "v_exp_f32 v49, v17\n"
    "v_mfma_f32_16x16x32_bf16 a[24:27], a[248:251], v[100:103], a[24:27]\n"
    "v_exp_f32 v50, v18\n"
    "v_cvt_pk_bf16_f32 v109, v74, v75\n"
    "v_mfma_f32_16x16x32_bf16 a[12:15], a[252:255], v[96:99], a[12:15]\n"
    "v_exp_f32 v51, v19\n"
    "v_add_f32 v133, v133, v78\n"
    "v_mfma_f32_16x16x32_bf16 a[28:31], a[252:255], v[100:103], a[28:31]\n"
    "s_waitcnt lgkmcnt(0)\n"
    "v_exp_f32 v52, v20\n"
    "v_mfma_f32_16x16x32_bf16 v[192:195], a[224:227], v[96:99], v[192:195]\n"
    "buffer_load_dwordx4 v146, s[52:55], s69 offen offset:2048 lds\n"
    "v_exp_f32 v53, v21\n"
    "v_mfma_f32_16x16x32_bf16 v[208:211], a[224:227], v[100:103], v[208:211]\n"
    "v_add_f32 v132, v132, v71\n"
    "v_cvt_pk_bf16_f32 v110, v76, v77\n"
    "v_mfma_f32_16x16x32_bf16 v[196:199], a[228:231], v[96:99], v[196:199]\n"
    "v_exp_f32 v54, v22\n"
    "v_add_f32 v133, v133, v79\n"
    "v_mfma_f32_16x16x32_bf16 v[212:215], a[228:231], v[100:103], v[212:215]\n"
    "v_exp_f32 v55, v23\n"
    "v_mfma_f32_16x16x32_bf16 v[200:203], a[232:235], v[96:99], v[200:203]\n"
    "buffer_load_dwordx4 v147, s[52:55], s69 offen offset:3072 lds\n"
    "v_max_f32 v15, v132, v133\n"
    "s_add_u32 s69, s69, 131072\n"
    "v_mfma_f32_16x16x32_bf16 v[216:219], a[232:235], v[100:103], v[216:219]\n"
    "v_exp_f32 v56, v24\n"
    "v_cvt_pk_bf16_f32 v111, v78, v79\n"
    "v_mfma_f32_16x16x32_bf16 v[204:207], a[236:239], v[96:99], v[204:207]\n"
    "v_cmp_nge_f32 vcc, s76, v15\n"
    "v_exp_f32 v57, v25\n"
    "v_mfma_f32_16x16x32_bf16 v[220:223], a[236:239], v[100:103], v[220:223]\n"
    "v_exp_f32 v58, v26\n"
    "s_cbranch_vccnz .Lrare_b2KV\n"
    ".Lback_b2KV:\n"
    "v_mfma_f32_16x16x32_bf16 v[32:35], a[192:195], a[160:163], v[88:91]\n"
    "s_add_u32 m0, s71, 49152\n"
    "v_exp_f32 v59, v27\n"
    "v_mfma_f32_16x16x32_bf16 v[40:43], a[192:195], a[176:179], v[92:95]\n"
    "buffer_load_dwordx4 v144, s[52:55], s68 offen offset:0 lds\n"
    "v_add_f32 v136, v136, v132\n"
    "v_mfma_f32_16x16x32_bf16 v[36:39], a[208:211], a[160:163], v[88:91]\n"
    "v_cvt_pk_bf16_f32 v96, v48, v49\n"
    "v_add_f32 v137, v137, v133\n"
    "v_mfma_f32_16x16x32_bf16 v[44:47], a[208:211], a[176:179], v[92:95]\n"
    "v_exp_f32 v60, v28\n"
    "v_mfma_f32_16x16x32_bf16 v[32:35], a[196:199], a[164:167], v[32:35]\n"
    "v_add_f32 v130, v48, v49\n"
    "v_cvt_pk_bf16_f32 v97, v50, v51\n"
    "v_mfma_f32_16x16x32_bf16 v[40:43], a[196:199], a[180:183], v[40:43]\n"
    "v_exp_f32 v61, v29\n"
    "v_mfma_f32_16x16x32_bf16 v[36:39], a[212:215], a[164:167], v[36:39]\n"
    "buffer_load_dwordx4 v145, s[52:55], s68 offen offset:1024 lds\n"
    "v_add_f32 v131, v56, v57\n"
    "v_mfma_f32_16x16x32_bf16 v[44:47], a[212:215], a[180:183], v[44:47]\n"
    "v_add_f32 v130, v130, v50\n"
    "v_exp_f32 v62, v30\n"
    "v_mfma_f32_16x16x32_bf16 v[32:35], a[200:203], a[168:171], v[32:35]\n"
    "v_cvt_pk_bf16_f32 v98, v52, v53\n"
    "v_add_f32 v131, v131, v58\n"
    "v_mfma_f32_16x16x32_bf16 v[40:43], a[200:203], a[184:187], v[40:43]\n"
    "v_exp_f32 v63, v31\n"
    "v_mfma_f32_16x16x32_bf16 v[36:39], a[216:219], a[168:171], v[36:39]\n"
    "buffer_load_dwordx4 v146, s[52:55], s68 offen offset:2048 lds\n"
    "v_add_f32 v130, v130, v51\n"
    "v_mfma_f32_16x16x32_bf16 v[44:47], a[216:219], a[184:187], v[44:47]\n"
    "v_add_f32 v131, v131, v59\n"
    "v_cvt_pk_bf16_f32 v99, v54, v55\n"
    "v_mfma_f32_16x16x32_bf16 v[32:35], a[204:207], a[172:175], v[32:35]\n"
    "v_add_f32 v130, v130, v52\n"
    "v_add_f32 v131, v131, v60\n"
    "v_mfma_f32_16x16x32_bf16 v[40:43], a[204:207], a[188:191], v[40:43]\n"
    "v_cvt_pk_bf16_f32 v100, v56, v57\n"
    "v_add_f32 v130, v130, v53\n"
    "v_mfma_f32_16x16x32_bf16 v[36:39], a[220:223], a[172:175], v[36:39]\n"
    "buffer_load_dwordx4 v147, s[52:55], s68 offen offset:3072 lds\n"
    "s_add_u32 s68, s68, 131072\n"
    "v_add_f32 v131, v131, v61\n"
    "v_cvt_pk_bf16_f32 v101, v58, v59\n"
    "v_mfma_f32_16x16x32_bf16 v[44:47], a[220:223], a[188:191], v[44:47]\n"
    "v_add_f32 v130, v130, v54\n"
    "v_add_f32 v131, v131, v62\n"
    "v_mfma_f32_16x16x32_bf16 a[32:35], a[240:243], v[104:107], a[32:35]\n"
    "ds_read_b128 a[192:195], v112 offset:0\n"
    "v_add_f32 v130, v130, v55\n"
    "v_mfma_f32_16x16x32_bf16 a[48:51], a[240:243], v[108:111], a[48:51]\n"
    "ds_read_b128 a[208:211], v112 offset:4096\n"
    "v_cvt_pk_bf16_f32 v102, v60, v61\n"
    "v_mfma_f32_16x16x32_bf16 a[36:39], a[244:247], v[104:107], a[36:39]\n"
    "ds_read_b128 a[196:199], v113 offset:0\n"
    "v_add_f32 v131, v131, v63\n"
    "v_mfma_f32_16x16x32_bf16 a[52:55], a[244:247], v[108:111], a[52:55]\n"
    "v_exp_f32 v64, v32\n"
    "v_mfma_f32_16x16x32_bf16 a[40:43], a[248:251], v[104:107], a[40:43]\n"
    "ds_read_b128 a[212:215], v113 offset:4096\n"
    "v_exp_f32 v65, v33\n"
    "v_mfma_f32_16x16x32_bf16 a[56:59], a[248:251], v[108:111], a[56:59]\n"
    "ds_read_b128 a[200:203], v114 offset:0\n"
    "v_exp_f32 v66, v34\n"
    "v_mfma_f32_16x16x32_bf16 a[44:47], a[252:255], v[104:107], a[44:47]\n"
    "ds_read_b128 a[216:219], v114 offset:4096\n"
    "v_exp_f32 v67, v35\n"
    "v_mfma_f32_16x16x32_bf16 a[60:63], a[252:255], v[108:111], a[60:63]\n"
    "v_exp_f32 v68, v36\n"
    "v_mfma_f32_16x16x32_bf16 v[224:227], a[224:227], v[104:107], v[224:227]\n"
    "ds_read_b128 a[204:207], v115 offset:0\n"
    "v_max_f32 v15, v130, v131\n"
    "v_mfma_f32_16x16x32_bf16 v[240:243], a[224:227], v[108:111], v[240:243]\n"
    "ds_read_b128 a[220:223], v115 offset:4096\n"
    "v_cvt_pk_bf16_f32 v103, v62, v63\n"
    "v_mfma_f32_16x16x32_bf16 v[228:231], a[228:231], v[104:107], v[228:231]\n"
    "v_exp_f32 v69, v37\n"
    "v_mfma_f32_16x16x32_bf16 v[244:247], a[228:231], v[108:111], v[244:247]\n"
    "v_exp_f32 v70, v38\n"
    "v_mfma_f32_16x16x32_bf16 v[232:235], a[232:235], v[104:107], v[232:235]\n"
    "v_exp_f32 v71, v39\n"
    "v_cmp_nge_f32 vcc, s76, v15\n"
    "v_mfma_f32_16x16x32_bf16 v[248:251], a[232:235], v[108:111], v[248:251]\n"
    "s_memrealtime s[34:35]\n"
    "v_exp_f32 v72, v40\n"
    "v_mfma_f32_16x16x32_bf16 v[236:239], a[236:239], v[104:107], v[236:239]\n"
    "v_exp_f32 v73, v41\n"
    "v_exp_f32 v74, v42\n"
    "v_mfma_f32_16x16x32_bf16 v[252:255], a[236:239], v[108:111], v[252:255]\n"
    "v_exp_f32 v75, v43\n"
    "s_cbranch_vccnz .Lrare_c\n"
    ".Lback_c:\n"
    "v_add_f32 v134, v134, v130\n"
    "v_add_f32 v135, v135, v131\n"
    "s_waitcnt vmcnt(0) lgkmcnt(0)\n"
    "s_barrier\n"
    "ds_read_b64_tr_b16 a[240:241], v120 offset:49152\n"
    "ds_read_b64_tr_b16 a[242:243], v120 offset:53248\n"
    "ds_read_b64_tr_b16 a[244:245], v121 offset:49152\n"
    "ds_read_b64_tr_b16 a[246:247], v121 offset:53248\n"
    "ds_read_b64_tr_b16 a[248:249], v122 offset:49152\n"
    "ds_read_b64_tr_b16 a[250:251], v122 offset:53248\n"
    "ds_read_b64_tr_b16 a[252:253], v123 offset:49152\n"
    "ds_read_b64_tr_b16 a[254:255], v123 offset:53248\n"
    "ds_read_b64_tr_b16 a[224:225], v116 offset:49152\n"
    "ds_read_b64_tr_b16 a[226:227], v116 offset:53248\n"
    "ds_read_b64_tr_b16 a[228:229], v117 offset:49152\n"
    "ds_read_b64_tr_b16 a[230:231], v117 offset:53248\n"
    "ds_read_b64_tr_b16 a[232:233], v118 offset:49152\n"
    "ds_read_b64_tr_b16 a[234:235], v118 offset:53248\n"
    "ds_read_b64_tr_b16 a[236:237], v119 offset:49152\n"
    "ds_read_b64_tr_b16 a[238:239], v119 offset:53248\n"
    "v_mov_b32 v8, -1\n"
    "v_mov_b32 v9, -1\n"
    "v_mov_b32 v10, -1\n"
    "v_mov_b32 v11, -1\n"
    "buffer_store_dwordx4 v[8:11], v153, s[48:51], s7 offen offset:1024 sc1\n"
    "buffer_store_dwordx4 v[8:11], v153, s[48:51], s8 offen offset:2048 sc1\n"
    "buffer_store_dwordx4 v[8:11], v153, s[48:51], s9 offen offset:3072 sc1\n"
    "v_cmp_ne_u32 vcc, s64, v156\n"
    "s_mov_b32 s77, 1\n"
    "s_cbranch_vccnz .Lno_pa\n"
    "s_cmp_eq_u32 s18, 0\n"
    "s_cbranch_scc1 .Lno_pa\n"
    "s_mov_b32 s77, 0\n"
    "buffer_load_dwordx4 v[184:187], v14, s[48:51], s6 offen offset:0 sc1\n"
    "buffer_load_dwordx4 v[188:191], v14, s[48:51], s6 offen offset:16 sc1\n"
    "buffer_load_dwordx4 v[16:19], v153, s[48:51], s4 offen offset:0 sc1\n"
    "buffer_load_dwordx4 v[20:23], v153, s[48:51], s4 offen offset:1024 sc1\n"
    "buffer_load_dwordx4 v[24:27], v153, s[48:51], s4 offen offset:2048 sc1\n"
    "buffer_load_dwordx4 v[28:31], v153, s[48:51], s4 offen offset:3072 sc1\n"
    "buffer_load_dwordx4 v[48:51], v153, s[48:51], s5 offen offset:0 sc1\n"
    "buffer_load_dwordx4 v[52:55], v153, s[48:51], s5 offen offset:1024 sc1\n"
    "buffer_load_dwordx4 v[56:59], v153, s[48:51], s5 offen offset:2048 sc1\n"
    "buffer_load_dwordx4 v[60:63], v153, s[48:51], s5 offen offset:3072 sc1\n"
    ".Lno_pa:\n"
    "v_exp_f32 v76, v44\n"
    "v_exp_f32 v77, v45\n"
    "v_exp_f32 v78, v46\n"
    "v_exp_f32 v79, v47\n"
    "v_add_f32 v132, v64, v65\n"
    "v_add_f32 v133, v72, v73\n"
    "v_add_f32 v132, v132, v66\n"
    "v_add_f32 v133, v133, v74\n"
    "v_add_f32 v132, v132, v67\n"
    "v_add_f32 v133, v133, v75\n"
    "v_add_f32 v132, v132, v68\n"
    "v_add_f32 v133, v133, v76\n"
    "s_waitcnt lgkmcnt(8)\n"
    "v_mfma_f32_16x16x32_bf16 a[0:3], a[240:243], v[96:99], a[0:3]\n"
    "v_add_f32 v132, v132, v69\n"
    "v_cvt_pk_bf16_f32 v104, v64, v65\n"
    "v_mfma_f32_16x16x32_bf16 a[16:19], a[240:243], v[100:103], a[16:19]\n"
    "v_add_f32 v133, v133, v77\n"
    "v_cvt_pk_bf16_f32 v105, v66, v67\n"
    "v_mfma_f32_16x16x32_bf16 a[4:7], a[244:247], v[96:99], a[4:7]\n"
    "v_add_f32 v132, v132, v70\n"
    "v_cvt_pk_bf16_f32 v106, v68, v69\n"
    "v_mfma_f32_16x16x32_bf16 a[20:23], a[244:247], v[100:103], a[20:23]\n"
    "v_add_f32 v133, v133, v78\n"
    "v_cvt_pk_bf16_f32 v107, v70, v71\n"
    "v_mfma_f32_16x16x32_bf16 a[8:11], a[248:251], v[96:99], a[8:11]\n"
    "v_add_f32 v132, v132, v71\n"
    "v_cvt_pk_bf16_f32 v108, v72, v73\n"
    "v_mfma_f32_16x16x32_bf16 a[24:27], a[248:251], v[100:103], a[24:27]\n"
    "v_add_f32 v133, v133, v79\n"
    "v_cvt_pk_bf16_f32 v109, v74, v75\n"
    "v_mfma_f32_16x16x32_bf16 a[12:15], a[252:255], v[96:99], a[12:15]\n"
    "v_max_f32 v15, v132, v133\n"
    "v_cvt_pk_bf16_f32 v110, v76, v77\n"
    "v_mfma_f32_16x16x32_bf16 a[28:31], a[252:255], v[100:103], a[28:31]\n"
    "s_waitcnt lgkmcnt(0)\n"
    "s_barrier\n"
    "v_cmp_nge_f32 vcc, s76, v15\n"
    "v_mfma_f32_16x16x32_bf16 v[192:195], a[224:227], v[96:99], v[192:195]\n"
    "v_cvt_pk_bf16_f32 v111, v78, v79\n"
    "v_mfma_f32_16x16x32_bf16 v[208:211], a[224:227], v[100:103], v[208:211]\n"
    "v_mfma_f32_16x16x32_bf16 v[196:199], a[228:231], v[96:99], v[196:199]\n"
    "v_mfma_f32_16x16x32_bf16 v[212:215], a[228:231], v[100:103], v[212:215]\n"
    "v_mfma_f32_16x16x32_bf16 v[200:203], a[232:235], v[96:99], v[200:203]\n"
    "v_mfma_f32_16x16x32_bf16 v[216:219], a[232:235], v[100:103], v[216:219]\n"
    "v_mfma_f32_16x16x32_bf16 v[204:207], a[236:239], v[96:99], v[204:207]\n"
    "v_mfma_f32_16x16x32_bf16 v[220:223], a[236:239], v[100:103], v[220:223]\n"
    "s_nop 0\n"
    "s_cbranch_vccnz .Lrare_d\n"
    ".Lback_d:\n"
    "v_add_f32 v136, v136, v132\n"
    "v_add_f32 v137, v137, v133\n"
    "v_mfma_f32_16x16x32_bf16 a[32:35], a[240:243], v[104:107], a[32:35]\n"
    "ds_write_b128 v161, a[0:3] offset:0\n"
    "v_mov_b32 v177, v134\n"
    "v_mov_b32 v179, v135\n"
    "v_mfma_f32_16x16x32_bf16 a[48:51], a[240:243], v[108:111], a[48:51]\n"
    "ds_write_b128 v161, a[4:7] offset:1024\n"
    "v_mov_b32 v181, v136\n"
    "v_mov_b32 v183, v137\n"
    "v_mfma_f32_16x16x32_bf16 a[36:39], a[244:247], v[104:107], a[36:39]\n"
    "ds_write_b128 v161, a[8:11] offset:2048\n"
    "v_permlane16_swap_b32 v177, v134\n"
    "v_permlane16_swap_b32 v179, v135\n"
    "v_mfma_f32_16x16x32_bf16 a[52:55], a[244:247], v[108:111], a[52:55]\n"
    "ds_write_b128 v161, a[12:15] offset:3072\n"
    "v_permlane16_swap_b32 v181, v136\n"
    "v_permlane16_swap_b32 v183, v137\n"
    "v_mfma_f32_16x16x32_bf16 a[40:43], a[248:251], v[104:107], a[40:43]\n"
    "ds_write_b128 v161, a[16:19] offset:4096\n"
    "v_add_f32 v134, v134, v177\n"
    "v_add_f32 v135, v135, v179\n"
    "v_mfma_f32_16x16x32_bf16 a[56:59], a[248:251], v[108:111], a[56:59]\n"
    "ds_write_b128 v161, a[20:23] offset:5120\n"
    "v_add_f32 v136, v136, v181\n"
    "v_add_f32 v137, v137, v183\n"
    "v_mfma_f32_16x16x32_bf16 a[44:47], a[252:255], v[104:107], a[44:47]\n"
    "ds_write_b128 v161, a[24:27] offset:6144\n"
    "v_mov_b32 v177, v134\n"
    "v_mov_b32 v179, v135\n"
    "v_mfma_f32_16x16x32_bf16 a[60:63], a[252:255], v[108:111], a[60:63]\n"
    "ds_write_b128 v161, a[28:31] offset:7168\n"
    "v_mov_b32 v181, v136\n"
    "v_mov_b32 v183, v137\n"
    "v_mfma_f32_16x16x32_bf16 v[224:227], a[224:227], v[104:107], v[224:227]\n"
    "ds_write_b128 v162, a[32:35] offset:0\n"
    "v_permlane32_swap_b32 v177, v134\n"
    "v_permlane32_swap_b32 v179, v135\n"
    "v_mfma_f32_16x16x32_bf16 v[240:243], a[224:227], v[108:111], v[240:243]\n"
    "ds_write_b128 v162, a[48:51] offset:4096\n"
    "v_permlane32_swap_b32 v181, v136\n"
    "v_permlane32_swap_b32 v183, v137\n"
    "v_mfma_f32_16x16x32_bf16 v[228:231], a[228:231], v[104:107], v[228:231]\n"
    "ds_write_b128 v162, a[36:39] offset:1024\n"
    "v_add_f32 v177, v134, v177\n"
    "v_add_f32 v179, v135, v179\n"
    "v_mfma_f32_16x16x32_bf16 v[244:247], a[228:231], v[108:111], v[244:247]\n"
    "ds_write_b128 v162, a[52:55] offset:5120\n"
    "v_add_f32 v181, v136, v181\n"
    "v_add_f32 v183, v137, v183\n"
    "v_mfma_f32_16x16x32_bf16 v[232:235], a[232:235], v[104:107], v[232:235]\n"
    "ds_write_b128 v162, a[40:43] offset:2048\n"
    "v_mov_b32 v176, v138\n"
    "v_mov_b32 v178, v139\n"
    "v_mfma_f32_16x16x32_bf16 v[248:251], a[232:235], v[108:111], v[248:251]\n"
    "ds_write_b128 v162, a[56:59] offset:6144\n"
    "v_mov_b32 v180, v140\n"
    "v_mov_b32 v182, v141\n"
    "v_mfma_f32_16x16x32_bf16 v[236:239], a[236:239], v[104:107], v[236:239]\n"
    "ds_write_b128 v162, a[44:47] offset:3072\n"
    "v_mfma_f32_16x16x32_bf16 v[252:255], a[236:239], v[108:111], v[252:255]\n"
    "ds_write_b128 v165, v[176:179] offset:0\n"
    "ds_write_b128 v162, a[60:63] offset:7168\n"
    "ds_write_b128 v165, v[180:183] offset:16\n"
    "s_waitcnt lgkmcnt(0)\n"
    "s_barrier\n"
    "ds_read_b128 v[168:171], v166 offset:0\n"
    "ds_read_b128 v[172:175], v166 offset:16\n"
    "ds_read_b128 v[80:83], v163 offset:0\n"
    "ds_read_b128 v[84:87], v163 offset:1024\n"
    "ds_read_b128 v[88:91], v163 offset:2048\n"
    "ds_read_b128 v[92:95], v163 offset:3072\n"
    "ds_read_b128 v[96:99], v163 offset:4096\n"
    "ds_read_b128 v[100:103], v163 offset:5120\n"
    "ds_read_b128 v[104:107], v163 offset:6144\n"
    "ds_read_b128 v[108:111], v163 offset:7168\n"
    "ds_read_b128 v[112:115], v164 offset:0\n"
    "ds_read_b128 v[116:119], v164 offset:1024\n"
    "ds_read_b128 v[120:123], v164 offset:2048\n"
    "ds_read_b128 v[124:127], v164 offset:3072\n"
    "ds_read_b128 v[128:131], v164 offset:4096\n"
    "s_waitcnt lgkmcnt(12)\n"
    "ds_read_b128 v[132:135], v164 offset:5120\n"
    "ds_read_b128 v[136:139], v164 offset:6144\n"
    "ds_read_b128 v[140:143], v164 offset:7168\n"
    "s_cmp_eq_u32 s18, 0\n"
    "s_cbranch_scc1 .Lcomb_a\n"
    "s_cmp_eq_u32 s77, 0\n"
    "s_cbranch_scc1 .Lpa_here\n"
    "v_mov_b32 v1, 0\n"
    "s_mov_b32 s79, 65536\n"
    ".Laf_poll:\n"
    "buffer_load_dword v3, v1, s[48:51], s13 offen sc1\n"
    "s_waitcnt vmcnt(0)\n"
    "v_cmp_ne_u32 vcc, s64, v3\n"
    "s_sub_u32 s79, s79, 1\n"
    "s_cbranch_vccz .Laf_done\n"
    "s_cbranch_scc0 .Laf_poll\n"
    ".Laf_done:\n"
    "buffer_load_dwordx4 v[184:187], v14, s[48:51], s6 offen offset:0 sc1\n"
    "buffer_load_dwordx4 v[188:191], v14, s[48:51], s6 offen offset:16 sc1\n"
    "buffer_load_dwordx4 v[16:19], v153, s[48:51], s4 offen offset:0 sc1\n"
    "buffer_load_dwordx4 v[20:23], v153, s[48:51], s4 offen offset:1024 sc1\n"
    "buffer_load_dwordx4 v[24:27], v153, s[48:51], s4 offen offset:2048 sc1\n"
    "buffer_load_dwordx4 v[28:31], v153, s[48:51], s4 offen offset:3072 sc1\n"
    "buffer_load_dwordx4 v[48:51], v153, s[48:51], s5 offen offset:0 sc1\n"
    "buffer_load_dwordx4 v[52:55], v153, s[48:51], s5 offen offset:1024 sc1\n"
    "buffer_load_dwordx4 v[56:59], v153, s[48:51], s5 offen offset:2048 sc1\n"
    "buffer_load_dwordx4 v[60:63], v153, s[48:51], s5 offen offset:3072 sc1\n"
    ".Lpa_here:\n"
    "s_waitcnt vmcnt(8)\n"
    "v_max3_f32 v12, v176, v168, v184\n"
    "v_max3_f32 v13, v178, v170, v186\n"
    "v_max3_f32 v14, v180, v172, v188\n"
    "v_max3_f32 v15, v182, v174, v190\n"
    "v_sub_f32 v176, v176, v12\n"
    "v_sub_f32 v178, v178, v13\n"
    "v_sub_f32 v180, v180, v14\n"
    "v_sub_f32 v182, v182, v15\n"
    "v_sub_f32 v168, v168, v12\n"
    "v_sub_f32 v170, v170, v13\n"
    "v_sub_f32 v172, v172, v14\n"
    "v_sub_f32 v174, v174, v15\n"
    "v_sub_f32 v184, v184, v12\n"
    "v_sub_f32 v186, v186, v13\n"
    "v_sub_f32 v188, v188, v14\n"
    "v_sub_f32 v190, v190, v15\n"
    "v_exp_f32 v176, v176\n"
    "v_exp_f32 v178, v178\n"
    "v_exp_f32 v180, v180\n"
    "v_exp_f32 v182, v182\n"
    "v_exp_f32 v168, v168\n"
    "v_exp_f32 v170, v170\n"
    "v_exp_f32 v172, v172\n"
    "v_exp_f32 v174, v174\n"
    "v_exp_f32 v184, v184\n"
    "v_exp_f32 v186, v186\n"
    "v_exp_f32 v188, v188\n"
    "v_exp_f32 v190, v190\n"
    "v_mul_f32 v12, v176, v177\n"
    "v_mul_f32 v13, v178, v179\n"
    "v_mul_f32 v14, v180, v181\n"
    "v_mul_f32 v15, v182, v183\n"
    "v_fmac_f32 v12, v168, v169\n"
    "v_fmac_f32 v13, v170, v171\n"
    "v_fmac_f32 v14, v172, v173\n"
    "v_fmac_f32 v15, v174, v175\n"
    "v_fmac_f32 v12, v184, v185\n"
    "v_fmac_f32 v13, v186, v187\n"
    "v_fmac_f32 v14, v188, v189\n"
    "v_fmac_f32 v15, v190, v191\n"
    "v_rcp_f32 v12, v12\n"
    "v_rcp_f32 v13, v13\n"
    "v_rcp_f32 v14, v14\n"
    "v_rcp_f32 v15, v15\n"
    "v_mul_f32 v4, v176, v12\n"
    "v_mul_f32 v5, v178, v13\n"
    "v_mul_f32 v6, v180, v14\n"
    "v_mul_f32 v7, v182, v15\n"
    "v_mul_f32 v8, v168, v12\n"
    "v_mul_f32 v9, v170, v13\n"
    "v_mul_f32 v10, v172, v14\n"
    "v_mul_f32 v11, v174, v15\n"
    "v_mul_f32 v12, v184, v12\n"
    "v_mul_f32 v13, v186, v13\n"
    "v_mul_f32 v14, v188, v14\n"
    "v_mul_f32 v15, v190, v15\n"
    "s_mov_b32 s70, 0xffff0000\n"
    "v_pk_mul_f32 v[192:193], v[192:193], v[4:5] op_sel:[0,0] op_sel_hi:[1,0]\n"
    "v_pk_mul_f32 v[194:195], v[194:195], v[4:5] op_sel:[0,0] op_sel_hi:[1,0]\n"
    "v_pk_mul_f32 v[196:197], v[196:197], v[4:5] op_sel:[0,0] op_sel_hi:[1,0]\n"
    "v_pk_mul_f32 v[198:199], v[198:199], v[4:5] op_sel:[0,0] op_sel_hi:[1,0]\n"
    "v_pk_mul_f32 v[200:201], v[200:201], v[4:5] op_sel:[0,0] op_sel_hi:[1,0]\n"
    "v_pk_mul_f32 v[202:203], v[202:203], v[4:5] op_sel:[0,0] op_sel_hi:[1,0]\n"
    "v_pk_mul_f32 v[204:205], v[204:205], v[4:5] op_sel:[0,0] op_sel_hi:[1,0]\n"
    "v_pk_mul_f32 v[206:207], v[206:207], v[4:5] op_sel:[0,0] op_sel_hi:[1,0]\n"
    "s_waitcnt lgkmcnt(12)\n"
    "v_pk_fma_f32 v[192:193], v[80:81], v[8:9], v[192:193] op_sel:[0,0,0] op_sel_hi:[1,0,1]\n"
    "v_pk_fma_f32 v[194:195], v[82:83], v[8:9], v[194:195] op_sel:[0,0,0] op_sel_hi:[1,0,1]\n"
    "v_pk_fma_f32 v[196:197], v[84:85], v[8:9], v[196:197] op_sel:[0,0,0] op_sel_hi:[1,0,1]\n"
    "v_pk_fma_f32 v[198:199], v[86:87], v[8:9], v[198:199] op_sel:[0,0,0] op_sel_hi:[1,0,1]\n"
    "v_pk_fma_f32 v[200:201], v[88:89], v[8:9], v[200:201] op_sel:[0,0,0] op_sel_hi:[1,0,1]\n"
    "v_pk_fma_f32 v[202:203], v[90:91], v[8:9], v[202:203] op_sel:[0,0,0] op_sel_hi:[1,0,1]\n"
    "v_pk_fma_f32 v[204:205], v[92:93], v[8:9], v[204:205] op_sel:[0,0,0] op_sel_hi:[1,0,1]\n"
    "v_pk_fma_f32 v[206:207], v[94:95], v[8:9], v[206:207] op_sel:[0,0,0] op_sel_hi:[1,0,1]\n"
    "s_waitcnt vmcnt(6)\n"
    "v_and_b32 v32, s70, v16\n"
    "v_and_b32 v33, s70, v17\n"
    "v_and_b32 v34, s70, v18\n"
    "v_and_b32 v35, s70, v19\n"
    "v_and_b32 v36, s70, v20\n"
    "v_and_b32 v37, s70, v21\n"
    "v_and_b32 v38, s70, v22\n"
    "v_and_b32 v39, s70, v23\n"
    "v_lshlrev_b32 v16, 16, v16\n"
    "v_lshlrev_b32 v17, 16, v17\n"
    "v_lshlrev_b32 v18, 16, v18\n"
    "v_lshlrev_b32 v19, 16, v19\n"
    "v_lshlrev_b32 v20, 16, v20\n"
    "v_lshlrev_b32 v21, 16, v21\n"
    "v_lshlrev_b32 v22, 16, v22\n"
    "v_lshlrev_b32 v23, 16, v23\n"
    "v_pk_fma_f32 v[192:193], v[16:17], v[12:13], v[192:193] op_sel:[0,0,0] op_sel_hi:[1,0,1]\n"
    "v_pk_fma_f32 v[194:195], v[18:19], v[12:13], v[194:195] op_sel:[0,0,0] op_sel_hi:[1,0,1]\n"
    "v_pk_fma_f32 v[196:197], v[20:21], v[12:13], v[196:197] op_sel:[0,0,0] op_sel_hi:[1,0,1]\n"
    "v_pk_fma_f32 v[198:199], v[22:23], v[12:13], v[198:199] op_sel:[0,0,0] op_sel_hi:[1,0,1]\n"
    "v_cvt_pk_bf16_f32 v80, v192, v193\n"
    "v_cvt_pk_bf16_f32 v81, v194, v195\n"
    "v_cvt_pk_bf16_f32 v82, v196, v197\n"
    "v_cvt_pk_bf16_f32 v83, v198, v199\n"
    "buffer_store_dwordx4 v[80:83], v153, s[48:51], s10 offen offset:0 sc1\n"
    "v_pk_fma_f32 v[200:201], v[32:33], v[12:13], v[200:201] op_sel:[0,0,0] op_sel_hi:[1,0,1]\n"
    "v_pk_fma_f32 v[202:203], v[34:35], v[12:13], v[202:203] op_sel:[0,0,0] op_sel_hi:[1,0,1]\n"
    "v_pk_fma_f32 v[204:205], v[36:37], v[12:13], v[204:205] op_sel:[0,0,0] op_sel_hi:[1,0,1]\n"
    "v_pk_fma_f32 v[206:207], v[38:39], v[12:13], v[206:207] op_sel:[0,0,0] op_sel_hi:[1,0,1]\n"
    "v_cvt_pk_bf16_f32 v84, v200, v201\n"
    "v_cvt_pk_bf16_f32 v85, v202, v203\n"
    "v_cvt_pk_bf16_f32 v86, v204, v205\n"
    "v_cvt_pk_bf16_f32 v87, v206, v207\n"
    "buffer_store_dwordx4 v[84:87], v153, s[48:51], s10 offen offset:1024 sc1\n"
    "s_add_u32 s10, s10, 4096\n"
    "v_pk_mul_f32 v[208:209], v[208:209], v[4:5] op_sel:[0,1] op_sel_hi:[1,1]\n"
    "v_pk_mul_f32 v[210:211], v[210:211], v[4:5] op_sel:[0,1] op_sel_hi:[1,1]\n"
    "v_pk_mul_f32 v[212:213], v[212:213], v[4:5] op_sel:[0,1] op_sel_hi:[1,1]\n"
    "v_pk_mul_f32 v[214:215], v[214:215], v[4:5] op_sel:[0,1] op_sel_hi:[1,1]\n"
    "v_pk_mul_f32 v[216:217], v[216:217], v[4:5] op_sel:[0,1] op_sel_hi:[1,1]\n"
    "v_pk_mul_f32 v[218:219], v[218:219], v[4:5] op_sel:[0,1] op_sel_hi:[1,1]\n"
    "v_pk_mul_f32 v[220:221], v[220:221], v[4:5] op_sel:[0,1] op_sel_hi:[1,1]\n"
    "v_pk_mul_f32 v[222:223], v[222:223], v[4:5] op_sel:[0,1] op_sel_hi:[1,1]\n"
    "s_waitcnt lgkmcnt(8)\n"
    "v_pk_fma_f32 v[208:209], v[96:97], v[8:9], v[208:209] op_sel:[0,1,0] op_sel_hi:[1,1,1]\n"
    "v_pk_fma_f32 v[210:211], v[98:99], v[8:9], v[210:211] op_sel:[0,1,0] op_sel_hi:[1,1,1]\n"
    "v_pk_fma_f32 v[212:213], v[100:101], v[8:9], v[212:213] op_sel:[0,1,0] op_sel_hi:[1,1,1]\n"
    "v_pk_fma_f32 v[214:215], v[102:103], v[8:9], v[214:215] op_sel:[0,1,0] op_sel_hi:[1,1,1]\n"
    "v_pk_fma_f32 v[216:217], v[104:105], v[8:9], v[216:217] op_sel:[0,1,0] op_sel_hi:[1,1,1]\n"
    "v_pk_fma_f32 v[218:219], v[106:107], v[8:9], v[218:219] op_sel:[0,1,0] op_sel_hi:[1,1,1]\n"
    "v_pk_fma_f32 v[220:221], v[108:109], v[8:9], v[220:221] op_sel:[0,1,0] op_sel_hi:[1,1,1]\n"
    "v_pk_fma_f32 v[222:223], v[110:111], v[8:9], v[222:223] op_sel:[0,1,0] op_sel_hi:[1,1,1]\n"
    "s_waitcnt vmcnt(6)\n"
    "v_and_b32 v32, s70, v24\n"
    "v_and_b32 v33, s70, v25\n"
    "v_and_b32 v34, s70, v26\n"
    "v_and_b32 v35, s70, v27\n"
    "v_and_b32 v36, s70, v28\n"
    "v_and_b32 v37, s70, v29\n"
    "v_and_b32 v38, s70, v30\n"
    "v_and_b32 v39, s70, v31\n"
    "v_lshlrev_b32 v24, 16, v24\n"
    "v_lshlrev_b32 v25, 16, v25\n"
    "v_lshlrev_b32 v26, 16, v26\n"
    "v_lshlrev_b32 v27, 16, v27\n"
    "v_lshlrev_b32 v28, 16, v28\n"
    "v_lshlrev_b32 v29, 16, v29\n"
    "v_lshlrev_b32 v30, 16, v30\n"
    "v_lshlrev_b32 v31, 16, v31\n"
    "v_pk_fma_f32 v[208:209], v[24:25], v[12:13], v[208:209] op_sel:[0,1,0] op_sel_hi:[1,1,1]\n"
    "v_pk_fma_f32 v[210:211], v[26:27], v[12:13], v[210:211] op_sel:[0,1,0] op_sel_hi:[1,1,1]\n"
    "v_pk_fma_f32 v[212:213], v[28:29], v[12:13], v[212:213] op_sel:[0,1,0] op_sel_hi:[1,1,1]\n"
    "v_pk_fma_f32 v[214:215], v[30:31], v[12:13], v[214:215] op_sel:[0,1,0] op_sel_hi:[1,1,1]\n"
    "v_cvt_pk_bf16_f32 v96, v208, v209\n"
    "v_cvt_pk_bf16_f32 v97, v210, v211\n"
    "v_cvt_pk_bf16_f32 v98, v212, v213\n"
    "v_cvt_pk_bf16_f32 v99, v214, v215\n"
    "buffer_store_dwordx4 v[96:99], v153, s[48:51], s10 offen offset:0 sc1\n"
    "v_pk_fma_f32 v[216:217], v[32:33], v[12:13], v[216:217] op_sel:[0,1,0] op_sel_hi:[1,1,1]\n"
    "v_pk_fma_f32 v[218:219], v[34:35], v[12:13], v[218:219] op_sel:[0,1,0] op_sel_hi:[1,1,1]\n"
    "v_pk_fma_f32 v[220:221], v[36:37], v[12:13], v[220:221] op_sel:[0,1,0] op_sel_hi:[1,1,1]\n"
    "v_pk_fma_f32 v[222:223], v[38:39], v[12:13], v[222:223] op_sel:[0,1,0] op_sel_hi:[1,1,1]\n"
    "v_cvt_pk_bf16_f32 v100, v216, v217\n"
    "v_cvt_pk_bf16_f32 v101, v218, v219\n"
    "v_cvt_pk_bf16_f32 v102, v220, v221\n"
    "v_cvt_pk_bf16_f32 v103, v222, v223\n"
    "buffer_store_dwordx4 v[100:103], v153, s[48:51], s10 offen offset:1024 sc1\n"
    "s_add_u32 s10, s10, 4096\n"
    "v_pk_mul_f32 v[224:225], v[224:225], v[6:7] op_sel:[0,0] op_sel_hi:[1,0]\n"
    "v_pk_mul_f32 v[226:227], v[226:227], v[6:7] op_sel:[0,0] op_sel_hi:[1,0]\n"
    "v_pk_mul_f32 v[228:229], v[228:229], v[6:7] op_sel:[0,0] op_sel_hi:[1,0]\n"
    "v_pk_mul_f32 v[230:231], v[230:231], v[6:7] op_sel:[0,0] op_sel_hi:[1,0]\n"
    "v_pk_mul_f32 v[232:233], v[232:233], v[6:7] op_sel:[0,0] op_sel_hi:[1,0]\n"
    "v_pk_mul_f32 v[234:235], v[234:235], v[6:7] op_sel:[0,0] op_sel_hi:[1,0]\n"
    "v_pk_mul_f32 v[236:237], v[236:237], v[6:7] op_sel:[0,0] op_sel_hi:[1,0]\n"
    "v_pk_mul_f32 v[238:239], v[238:239], v[6:7] op_sel:[0,0] op_sel_hi:[1,0]\n"
    "s_waitcnt lgkmcnt(4)\n"
    "v_pk_fma_f32 v[224:225], v[112:113], v[10:11], v[224:225] op_sel:[0,0,0] op_sel_hi:[1,0,1]\n"
    "v_pk_fma_f32 v[226:227], v[114:115], v[10:11], v[226:227] op_sel:[0,0,0] op_sel_hi:[1,0,1]\n"
    "v_pk_fma_f32 v[228:229], v[116:117], v[10:11], v[228:229] op_sel:[0,0,0] op_sel_hi:[1,0,1]\n"
    "v_pk_fma_f32 v[230:231], v[118:119], v[10:11], v[230:231] op_sel:[0,0,0] op_sel_hi:[1,0,1]\n"
    "v_pk_fma_f32 v[232:233], v[120:121], v[10:11], v[232:233] op_sel:[0,0,0] op_sel_hi:[1,0,1]\n"
    "v_pk_fma_f32 v[234:235], v[122:123], v[10:11], v[234:235] op_sel:[0,0,0] op_sel_hi:[1,0,1]\n"
    "v_pk_fma_f32 v[236:237], v[124:125], v[10:11], v[236:237] op_sel:[0,0,0] op_sel_hi:[1,0,1]\n"
    "v_pk_fma_f32 v[238:239], v[126:127], v[10:11], v[238:239] op_sel:[0,0,0] op_sel_hi:[1,0,1]\n"
    "s_waitcnt vmcnt(6)\n"
    "v_and_b32 v32, s70, v48\n"
    "v_and_b32 v33, s70, v49\n"
    "v_and_b32 v34, s70, v50\n"
    "v_and_b32 v35, s70, v51\n"
    "v_and_b32 v36, s70, v52\n"
    "v_and_b32 v37, s70, v53\n"
    "v_and_b32 v38, s70, v54\n"
    "v_and_b32 v39, s70, v55\n"
    "v_lshlrev_b32 v48, 16, v48\n"
    "v_lshlrev_b32 v49, 16, v49\n"
    "v_lshlrev_b32 v50, 16, v50\n"
    "v_lshlrev_b32 v51, 16, v51\n"
    "v_lshlrev_b32 v52, 16, v52\n"
    "v_lshlrev_b32 v53, 16, v53\n"
    "v_lshlrev_b32 v54, 16, v54\n"
    "v_lshlrev_b32 v55, 16, v55\n"
    "v_pk_fma_f32 v[224:225], v[48:49], v[14:15], v[224:225] op_sel:[0,0,0] op_sel_hi:[1,0,1]\n"
    "v_pk_fma_f32 v[226:227], v[50:51], v[14:15], v[226:227] op_sel:[0,0,0] op_sel_hi:[1,0,1]\n"
    "v_pk_fma_f32 v[228:229], v[52:53], v[14:15], v[228:229] op_sel:[0,0,0] op_sel_hi:[1,0,1]\n"
    "v_pk_fma_f32 v[230:231], v[54:55], v[14:15], v[230:231] op_sel:[0,0,0] op_sel_hi:[1,0,1]\n"
    "v_cvt_pk_bf16_f32 v112, v224, v225\n"
    "v_cvt_pk_bf16_f32 v113, v226, v227\n"
    "v_cvt_pk_bf16_f32 v114, v228, v229\n"
    "v_cvt_pk_bf16_f32 v115, v230, v231\n"
    "buffer_store_dwordx4 v[112:115], v153, s[48:51], s10 offen offset:0 sc1\n"
    "v_pk_fma_f32 v[232:233], v[32:33], v[14:15], v[232:233] op_sel:[0,0,0] op_sel_hi:[1,0,1]\n"
    "v_pk_fma_f32 v[234:235], v[34:35], v[14:15], v[234:235] op_sel:[0,0,0] op_sel_hi:[1,0,1]\n"
    "v_pk_fma_f32 v[236:237], v[36:37], v[14:15], v[236:237] op_sel:[0,0,0] op_sel_hi:[1,0,1]\n"
    "v_pk_fma_f32 v[238:239], v[38:39], v[14:15], v[238:239] op_sel:[0,0,0] op_sel_hi:[1,0,1]\n"
    "v_cvt_pk_bf16_f32 v116, v232, v233\n"
    "v_cvt_pk_bf16_f32 v117, v234, v235\n"
    "v_cvt_pk_bf16_f32 v118, v236, v237\n"
    "v_cvt_pk_bf16_f32 v119, v238, v239\n"
    "buffer_store_dwordx4 v[116:119], v153, s[48:51], s10 offen offset:1024 sc1\n"
    "s_add_u32 s10, s10, 4096\n"
    "v_pk_mul_f32 v[240:241], v[240:241], v[6:7] op_sel:[0,1] op_sel_hi:[1,1]\n"
    "v_pk_mul_f32 v[242:243], v[242:243], v[6:7] op_sel:[0,1] op_sel_hi:[1,1]\n"
    "v_pk_mul_f32 v[244:245], v[244:245], v[6:7] op_sel:[0,1] op_sel_hi:[1,1]\n"
    "v_pk_mul_f32 v[246:247], v[246:247], v[6:7] op_sel:[0,1] op_sel_hi:[1,1]\n"
    "v_pk_mul_f32 v[248:249], v[248:249], v[6:7] op_sel:[0,1] op_sel_hi:[1,1]\n"
    "v_pk_mul_f32 v[250:251], v[250:251], v[6:7] op_sel:[0,1] op_sel_hi:[1,1]\n"
    "v_pk_mul_f32 v[252:253], v[252:253], v[6:7] op_sel:[0,1] op_sel_hi:[1,1]\n"
    "v_pk_mul_f32 v[254:255], v[254:255], v[6:7] op_sel:[0,1] op_sel_hi:[1,1]\n"
    "s_waitcnt lgkmcnt(0)\n"
    "s_barrier\n"
    "v_pk_fma_f32 v[240:241], v[128:129], v[10:11], v[240:241] op_sel:[0,1,0] op_sel_hi:[1,1,1]\n"
    "v_pk_fma_f32 v[242:243], v[130:131], v[10:11], v[242:243] op_sel:[0,1,0] op_sel_hi:[1,1,1]\n"
    "v_pk_fma_f32 v[244:245], v[132:133], v[10:11], v[244:245] op_sel:[0,1,0] op_sel_hi:[1,1,1]\n"
    "v_pk_fma_f32 v[246:247], v[134:135], v[10:11], v[246:247] op_sel:[0,1,0] op_sel_hi:[1,1,1]\n"
    "v_pk_fma_f32 v[248:249], v[136:137], v[10:11], v[248:249] op_sel:[0,1,0] op_sel_hi:[1,1,1]\n"
    "v_pk_fma_f32 v[250:251], v[138:139], v[10:11], v[250:251] op_sel:[0,1,0] op_sel_hi:[1,1,1]\n"
    "v_pk_fma_f32 v[252:253], v[140:141], v[10:11], v[252:253] op_sel:[0,1,0] op_sel_hi:[1,1,1]\n"
    "v_pk_fma_f32 v[254:255], v[142:143], v[10:11], v[254:255] op_sel:[0,1,0] op_sel_hi:[1,1,1]\n"
    "s_waitcnt vmcnt(6)\n"
    "v_and_b32 v32, s70, v56\n"
    "v_and_b32 v33, s70, v57\n"
    "v_and_b32 v34, s70, v58\n"
    "v_and_b32 v35, s70, v59\n"
    "v_and_b32 v36, s70, v60\n"
    "v_and_b32 v37, s70, v61\n"
    "v_and_b32 v38, s70, v62\n"
    "v_and_b32 v39, s70, v63\n"
    "v_lshlrev_b32 v56, 16, v56\n"
    "v_lshlrev_b32 v57, 16, v57\n"
    "v_lshlrev_b32 v58, 16, v58\n"
    "v_lshlrev_b32 v59, 16, v59\n"
    "v_lshlrev_b32 v60, 16, v60\n"
    "v_lshlrev_b32 v61, 16, v61\n"
    "v_lshlrev_b32 v62, 16, v62\n"
    "v_lshlrev_b32 v63, 16, v63\n"
    "v_pk_fma_f32 v[240:241], v[56:57], v[14:15], v[240:241] op_sel:[0,1,0] op_sel_hi:[1,1,1]\n"
    "v_pk_fma_f32 v[242:243], v[58:59], v[14:15], v[242:243] op_sel:[0,1,0] op_sel_hi:[1,1,1]\n"
    "v_pk_fma_f32 v[244:245], v[60:61], v[14:15], v[244:245] op_sel:[0,1,0] op_sel_hi:[1,1,1]\n"
    "v_pk_fma_f32 v[246:247], v[62:63], v[14:15], v[246:247] op_sel:[0,1,0] op_sel_hi:[1,1,1]\n"
    "v_cvt_pk_bf16_f32 v128, v240, v241\n"
    "v_cvt_pk_bf16_f32 v129, v242, v243\n"
    "v_cvt_pk_bf16_f32 v130, v244, v245\n"
    "v_cvt_pk_bf16_f32 v131, v246, v247\n"
    "buffer_store_dwordx4 v[128:131], v153, s[48:51], s10 offen offset:0 sc1\n"
    "v_pk_fma_f32 v[248:249], v[32:33], v[14:15], v[248:249] op_sel:[0,1,0] op_sel_hi:[1,1,1]\n"
    "v_pk_fma_f32 v[250:251], v[34:35], v[14:15], v[250:251] op_sel:[0,1,0] op_sel_hi:[1,1,1]\n"
    "v_pk_fma_f32 v[252:253], v[36:37], v[14:15], v[252:253] op_sel:[0,1,0] op_sel_hi:[1,1,1]\n"
    "v_pk_fma_f32 v[254:255], v[38:39], v[14:15], v[254:255] op_sel:[0,1,0] op_sel_hi:[1,1,1]\n"
    "v_cvt_pk_bf16_f32 v132, v248, v249\n"
    "v_cvt_pk_bf16_f32 v133, v250, v251\n"
    "v_cvt_pk_bf16_f32 v134, v252, v253\n"
    "v_cvt_pk_bf16_f32 v135, v254, v255\n"
    "buffer_store_dwordx4 v[132:135], v153, s[48:51], s10 offen offset:1024 sc1\n"
    "s_branch .Lcomb_done\n"
    ".Lcomb_a:\n"
    "v_max_f32 v184, v176, v168\n"
    "v_max_f32 v186, v178, v170\n"
    "v_max_f32 v188, v180, v172\n"
    "v_max_f32 v190, v182, v174\n"
    "v_sub_f32 v4, v176, v184\n"
    "v_sub_f32 v5, v178, v186\n"
    "v_sub_f32 v6, v180, v188\n"
    "v_sub_f32 v7, v182, v190\n"
    "v_sub_f32 v8, v168, v184\n"
    "v_sub_f32 v9, v170, v186\n"
    "v_sub_f32 v10, v172, v188\n"
    "v_sub_f32 v11, v174, v190\n"
    "v_exp_f32 v4, v4\n"
    "v_exp_f32 v5, v5\n"
    "v_exp_f32 v6, v6\n"
    "v_exp_f32 v7, v7\n"
    "v_exp_f32 v8, v8\n"
    "v_exp_f32 v9, v9\n"
    "v_exp_f32 v10, v10\n"
    "v_exp_f32 v11, v11\n"
    "v_mul_f32 v185, v4, v177\n"
    "v_mul_f32 v187, v5, v179\n"
    "v_mul_f32 v189, v6, v181\n"
    "v_mul_f32 v191, v7, v183\n"
    "v_fmac_f32 v185, v8, v169\n"
    "v_fmac_f32 v187, v9, v171\n"
    "v_fmac_f32 v189, v10, v173\n"
    "v_fmac_f32 v191, v11, v175\n"
    "buffer_store_dwordx4 v[184:187], v14, s[48:51], s6 offen offset:0 sc1\n"
    "buffer_store_dwordx4 v[188:191], v14, s[48:51], s6 offen offset:16 sc1\n"
    "v_pk_mul_f32 v[192:193], v[192:193], v[4:5] op_sel:[0,0] op_sel_hi:[1,0]\n"
    "v_pk_mul_f32 v[194:195], v[194:195], v[4:5] op_sel:[0,0] op_sel_hi:[1,0]\n"
    "v_pk_mul_f32 v[196:197], v[196:197], v[4:5] op_sel:[0,0] op_sel_hi:[1,0]\n"
    "v_pk_mul_f32 v[198:199], v[198:199], v[4:5] op_sel:[0,0] op_sel_hi:[1,0]\n"
    "v_pk_mul_f32 v[200:201], v[200:201], v[4:5] op_sel:[0,0] op_sel_hi:[1,0]\n"
    "v_pk_mul_f32 v[202:203], v[202:203], v[4:5] op_sel:[0,0] op_sel_hi:[1,0]\n"
    "v_pk_mul_f32 v[204:205], v[204:205], v[4:5] op_sel:[0,0] op_sel_hi:[1,0]\n"
    "v_pk_mul_f32 v[206:207], v[206:207], v[4:5] op_sel:[0,0] op_sel_hi:[1,0]\n"
    "s_waitcnt lgkmcnt(12)\n"
    "v_pk_fma_f32 v[192:193], v[80:81], v[8:9], v[192:193] op_sel:[0,0,0] op_sel_hi:[1,0,1]\n"
    "v_pk_fma_f32 v[194:195], v[82:83], v[8:9], v[194:195] op_sel:[0,0,0] op_sel_hi:[1,0,1]\n"
    "v_pk_fma_f32 v[196:197], v[84:85], v[8:9], v[196:197] op_sel:[0,0,0] op_sel_hi:[1,0,1]\n"
    "v_pk_fma_f32 v[198:199], v[86:87], v[8:9], v[198:199] op_sel:[0,0,0] op_sel_hi:[1,0,1]\n"
    "v_pk_fma_f32 v[200:201], v[88:89], v[8:9], v[200:201] op_sel:[0,0,0] op_sel_hi:[1,0,1]\n"
    "v_pk_fma_f32 v[202:203], v[90:91], v[8:9], v[202:203] op_sel:[0,0,0] op_sel_hi:[1,0,1]\n"
    "v_pk_fma_f32 v[204:205], v[92:93], v[8:9], v[204:205] op_sel:[0,0,0] op_sel_hi:[1,0,1]\n"
    "v_pk_fma_f32 v[206:207], v[94:95], v[8:9], v[206:207] op_sel:[0,0,0] op_sel_hi:[1,0,1]\n"
    "v_cvt_pk_bf16_f32 v16, v192, v200\n"
    "v_cvt_pk_bf16_f32 v17, v193, v201\n"
    "v_cvt_pk_bf16_f32 v18, v194, v202\n"
    "v_cvt_pk_bf16_f32 v19, v195, v203\n"
    "buffer_store_dwordx4 v[16:19], v153, s[48:51], s4 offen offset:0 sc1\n"
    "v_cvt_pk_bf16_f32 v20, v196, v204\n"
    "v_cvt_pk_bf16_f32 v21, v197, v205\n"
    "v_cvt_pk_bf16_f32 v22, v198, v206\n"
    "v_cvt_pk_bf16_f32 v23, v199, v207\n"
    "buffer_store_dwordx4 v[20:23], v153, s[48:51], s4 offen offset:1024 sc1\n"
    "v_pk_mul_f32 v[208:209], v[208:209], v[4:5] op_sel:[0,1] op_sel_hi:[1,1]\n"
    "v_pk_mul_f32 v[210:211], v[210:211], v[4:5] op_sel:[0,1] op_sel_hi:[1,1]\n"
    "v_pk_mul_f32 v[212:213], v[212:213], v[4:5] op_sel:[0,1] op_sel_hi:[1,1]\n"
    "v_pk_mul_f32 v[214:215], v[214:215], v[4:5] op_sel:[0,1] op_sel_hi:[1,1]\n"
    "v_pk_mul_f32 v[216:217], v[216:217], v[4:5] op_sel:[0,1] op_sel_hi:[1,1]\n"
    "v_pk_mul_f32 v[218:219], v[218:219], v[4:5] op_sel:[0,1] op_sel_hi:[1,1]\n"
    "v_pk_mul_f32 v[220:221], v[220:221], v[4:5] op_sel:[0,1] op_sel_hi:[1,1]\n"
    "v_pk_mul_f32 v[222:223], v[222:223], v[4:5] op_sel:[0,1] op_sel_hi:[1,1]\n"
    "s_waitcnt lgkmcnt(8)\n"
    "v_pk_fma_f32 v[208:209], v[96:97], v[8:9], v[208:209] op_sel:[0,1,0] op_sel_hi:[1,1,1]\n"
    "v_pk_fma_f32 v[210:211], v[98:99], v[8:9], v[210:211] op_sel:[0,1,0] op_sel_hi:[1,1,1]\n"
    "v_pk_fma_f32 v[212:213], v[100:101], v[8:9], v[212:213] op_sel:[0,1,0] op_sel_hi:[1,1,1]\n"
    "v_pk_fma_f32 v[214:215], v[102:103], v[8:9], v[214:215] op_sel:[0,1,0] op_sel_hi:[1,1,1]\n"
    "v_pk_fma_f32 v[216:217], v[104:105], v[8:9], v[216:217] op_sel:[0,1,0] op_sel_hi:[1,1,1]\n"
    "v_pk_fma_f32 v[218:219], v[106:107], v[8:9], v[218:219] op_sel:[0,1,0] op_sel_hi:[1,1,1]\n"
    "v_pk_fma_f32 v[220:221], v[108:109], v[8:9], v[220:221] op_sel:[0,1,0] op_sel_hi:[1,1,1]\n"
    "v_pk_fma_f32 v[222:223], v[110:111], v[8:9], v[222:223] op_sel:[0,1,0] op_sel_hi:[1,1,1]\n"
    "v_cvt_pk_bf16_f32 v24, v208, v216\n"
    "v_cvt_pk_bf16_f32 v25, v209, v217\n"
    "v_cvt_pk_bf16_f32 v26, v210, v218\n"
    "v_cvt_pk_bf16_f32 v27, v211, v219\n"
    "buffer_store_dwordx4 v[24:27], v153, s[48:51], s4 offen offset:2048 sc1\n"
    "v_cvt_pk_bf16_f32 v28, v212, v220\n"
    "v_cvt_pk_bf16_f32 v29, v213, v221\n"
    "v_cvt_pk_bf16_f32 v30, v214, v222\n"
    "v_cvt_pk_bf16_f32 v31, v215, v223\n"
    "buffer_store_dwordx4 v[28:31], v153, s[48:51], s4 offen offset:3072 sc1\n"
    "v_pk_mul_f32 v[224:225], v[224:225], v[6:7] op_sel:[0,0] op_sel_hi:[1,0]\n"
    "v_pk_mul_f32 v[226:227], v[226:227], v[6:7] op_sel:[0,0] op_sel_hi:[1,0]\n"
    "v_pk_mul_f32 v[228:229], v[228:229], v[6:7] op_sel:[0,0] op_sel_hi:[1,0]\n"
    "v_pk_mul_f32 v[230:231], v[230:231], v[6:7] op_sel:[0,0] op_sel_hi:[1,0]\n"
    "v_pk_mul_f32 v[232:233], v[232:233], v[6:7] op_sel:[0,0] op_sel_hi:[1,0]\n"
    "v_pk_mul_f32 v[234:235], v[234:235], v[6:7] op_sel:[0,0] op_sel_hi:[1,0]\n"
    "v_pk_mul_f32 v[236:237], v[236:237], v[6:7] op_sel:[0,0] op_sel_hi:[1,0]\n"
    "v_pk_mul_f32 v[238:239], v[238:239], v[6:7] op_sel:[0,0] op_sel_hi:[1,0]\n"
    "s_waitcnt lgkmcnt(4)\n"
    "v_pk_fma_f32 v[224:225], v[112:113], v[10:11], v[224:225] op_sel:[0,0,0] op_sel_hi:[1,0,1]\n"
    "v_pk_fma_f32 v[226:227], v[114:115], v[10:11], v[226:227] op_sel:[0,0,0] op_sel_hi:[1,0,1]\n"
    "v_pk_fma_f32 v[228:229], v[116:117], v[10:11], v[228:229] op_sel:[0,0,0] op_sel_hi:[1,0,1]\n"
    "v_pk_fma_f32 v[230:231], v[118:119], v[10:11], v[230:231] op_sel:[0,0,0] op_sel_hi:[1,0,1]\n"
    "v_pk_fma_f32 v[232:233], v[120:121], v[10:11], v[232:233] op_sel:[0,0,0] op_sel_hi:[1,0,1]\n"
    "v_pk_fma_f32 v[234:235], v[122:123], v[10:11], v[234:235] op_sel:[0,0,0] op_sel_hi:[1,0,1]\n"
    "v_pk_fma_f32 v[236:237], v[124:125], v[10:11], v[236:237] op_sel:[0,0,0] op_sel_hi:[1,0,1]\n"
    "v_pk_fma_f32 v[238:239], v[126:127], v[10:11], v[238:239] op_sel:[0,0,0] op_sel_hi:[1,0,1]\n"
    "v_cvt_pk_bf16_f32 v48, v224, v232\n"
    "v_cvt_pk_bf16_f32 v49, v225, v233\n"
    "v_cvt_pk_bf16_f32 v50, v226, v234\n"
    "v_cvt_pk_bf16_f32 v51, v227, v235\n"
    "buffer_store_dwordx4 v[48:51], v153, s[48:51], s5 offen offset:0 sc1\n"
    "v_cvt_pk_bf16_f32 v52, v228, v236\n"
    "v_cvt_pk_bf16_f32 v53, v229, v237\n"
    "v_cvt_pk_bf16_f32 v54, v230, v238\n"
    "v_cvt_pk_bf16_f32 v55, v231, v239\n"
    "buffer_store_dwordx4 v[52:55], v153, s[48:51], s5 offen offset:1024 sc1\n"
    "v_pk_mul_f32 v[240:241], v[240:241], v[6:7] op_sel:[0,1] op_sel_hi:[1,1]\n"
    "v_pk_mul_f32 v[242:243], v[242:243], v[6:7] op_sel:[0,1] op_sel_hi:[1,1]\n"
    "v_pk_mul_f32 v[244:245], v[244:245], v[6:7] op_sel:[0,1] op_sel_hi:[1,1]\n"
    "v_pk_mul_f32 v[246:247], v[246:247], v[6:7] op_sel:[0,1] op_sel_hi:[1,1]\n"
    "v_pk_mul_f32 v[248:249], v[248:249], v[6:7] op_sel:[0,1] op_sel_hi:[1,1]\n"
    "v_pk_mul_f32 v[250:251], v[250:251], v[6:7] op_sel:[0,1] op_sel_hi:[1,1]\n"
    "v_pk_mul_f32 v[252:253], v[252:253], v[6:7] op_sel:[0,1] op_sel_hi:[1,1]\n"
    "v_pk_mul_f32 v[254:255], v[254:255], v[6:7] op_sel:[0,1] op_sel_hi:[1,1]\n"
    "s_waitcnt lgkmcnt(0)\n"
    "s_barrier\n"
    "v_pk_fma_f32 v[240:241], v[128:129], v[10:11], v[240:241] op_sel:[0,1,0] op_sel_hi:[1,1,1]\n"
    "v_pk_fma_f32 v[242:243], v[130:131], v[10:11], v[242:243] op_sel:[0,1,0] op_sel_hi:[1,1,1]\n"
    "v_pk_fma_f32 v[244:245], v[132:133], v[10:11], v[244:245] op_sel:[0,1,0] op_sel_hi:[1,1,1]\n"
    "v_pk_fma_f32 v[246:247], v[134:135], v[10:11], v[246:247] op_sel:[0,1,0] op_sel_hi:[1,1,1]\n"
    "v_pk_fma_f32 v[248:249], v[136:137], v[10:11], v[248:249] op_sel:[0,1,0] op_sel_hi:[1,1,1]\n"
    "v_pk_fma_f32 v[250:251], v[138:139], v[10:11], v[250:251] op_sel:[0,1,0] op_sel_hi:[1,1,1]\n"
    "v_pk_fma_f32 v[252:253], v[140:141], v[10:11], v[252:253] op_sel:[0,1,0] op_sel_hi:[1,1,1]\n"
    "v_pk_fma_f32 v[254:255], v[142:143], v[10:11], v[254:255] op_sel:[0,1,0] op_sel_hi:[1,1,1]\n"
    "v_cvt_pk_bf16_f32 v56, v240, v248\n"
    "v_cvt_pk_bf16_f32 v57, v241, v249\n"
    "v_cvt_pk_bf16_f32 v58, v242, v250\n"
    "v_cvt_pk_bf16_f32 v59, v243, v251\n"
    "buffer_store_dwordx4 v[56:59], v153, s[48:51], s5 offen offset:2048 sc1\n"
    "v_cvt_pk_bf16_f32 v60, v244, v252\n"
    "v_cvt_pk_bf16_f32 v61, v245, v253\n"
    "v_cvt_pk_bf16_f32 v62, v246, v254\n"
    "v_cvt_pk_bf16_f32 v63, v247, v255\n"
    "buffer_store_dwordx4 v[60:63], v153, s[48:51], s5 offen offset:3072 sc1\n"
    ".Lcomb_done:\n"
    "s_add_u32 m0, s71, 40960\n"
    "s_nop 0\n"
    "buffer_load_dwordx4 v144, s[52:55], s69 offen offset:0 lds\n"
    "buffer_load_dwordx4 v145, s[52:55], s69 offen offset:1024 lds\n"
    "buffer_load_dwordx4 v146, s[52:55], s69 offen offset:2048 lds\n"
    "buffer_load_dwordx4 v147, s[52:55], s69 offen offset:3072 lds\n"
    "s_add_u32 s69, s69, 131072\n"
    "s_add_u32 m0, s71, 57344\n"
    "s_nop 0\n"
    "buffer_load_dwordx4 v144, s[52:55], s69 offen offset:0 lds\n"
    "buffer_load_dwordx4 v145, s[52:55], s69 offen offset:1024 lds\n"
    "buffer_load_dwordx4 v146, s[52:55], s69 offen offset:2048 lds\n"
    "buffer_load_dwordx4 v147, s[52:55], s69 offen offset:3072 lds\n"
    "s_add_u32 s69, s69, 131072\n"
    "s_add_u32 m0, s71, 24576\n"
    "s_sub_u32 s30, s69, 0x60000\n"
    "buffer_load_dwordx4 v147, s[52:55], s30 offen offset:3072 lds\n"
    "v_mov_b32 v3, s64\n"
    "v_mov_b32 v1, 0\n"
    "s_mov_b32 s28, s11\n"
    "v_xor_b32 v96, 0, v154\n"
    "v_xor_b32 v97, 32, v154\n"
    "v_xor_b32 v98, 64, v154\n"
    "v_xor_b32 v99, 96, v154\n"
    "v_xor_b32 v100, 65536, v154\n"
    "v_xor_b32 v101, 65568, v154\n"
    "v_xor_b32 v102, 65600, v154\n"
    "v_xor_b32 v103, 65632, v154\n"
    "s_waitcnt vmcnt(9)\n"
    "s_mov_b64 exec, 1\n"
    "buffer_store_dword v3, v1, s[48:51], s28 offen sc1\n"
    "s_mov_b64 exec, -1\n"
    "s_or_b32 s28, s24, s25\n"
    "s_cmp_lg_u32 s28, 0\n"
    "s_cbranch_scc1 .Lnobump\n"
    "v_lshlrev_b32 v1, 8, v148\n"
    "s_mov_b32 s79, 65536\n"
    ".Le_poll:\n"
    "s_mov_b32 s28, 4096\n"
    "buffer_load_dword v4, v1, s[48:51], s28 offen sc1\n"
    "s_mov_b32 s28, 20480\n"
    "buffer_load_dword v5, v1, s[48:51], s28 offen sc1\n"
    "s_mov_b32 s28, 36864\n"
    "buffer_load_dword v6, v1, s[48:51], s28 offen sc1\n"
    "s_mov_b32 s28, 53248\n"
    "buffer_load_dword v7, v1, s[48:51], s28 offen sc1\n"
    "s_waitcnt vmcnt(0)\n"
    "v_xor_b32 v4, s64, v4\n"
    "v_xor_b32 v5, s64, v5\n"
    "v_xor_b32 v6, s64, v6\n"
    "v_xor_b32 v7, s64, v7\n"
    "v_or3_b32 v4, v4, v5, v6\n"
    "v_or_b32 v4, v4, v7\n"
    "v_cmp_ne_u32 vcc, 0, v4\n"
    "s_sub_u32 s79, s79, 1\n"
    "s_cbranch_vccz .Le_done\n"
    "s_cbranch_scc0 .Le_poll\n"
    ".Le_done:\n"
    "v_mov_b32 v3, s64\n"
    "v_mov_b32 v1, 0\n"
    "s_mov_b64 exec, 1\n"
    "buffer_store_dword v3, v1, s[48:51], 0 offen sc1\n"
    "s_mov_b64 exec, -1\n"
    ".Lnobump:\n"
    "s_waitcnt vmcnt(1)\n"
    "s_barrier\n"
    "ds_read_b64_tr_b16 v[48:49], v96 offset:0\n"
    "ds_read_b64_tr_b16 v[50:51], v96 offset:2048\n"
    "ds_read_b64_tr_b16 v[52:53], v97 offset:0\n"
    "ds_read_b64_tr_b16 v[54:55], v97 offset:2048\n"
    "ds_read_b64_tr_b16 v[56:57], v98 offset:0\n"
    "ds_read_b64_tr_b16 v[58:59], v98 offset:2048\n"
    "ds_read_b64_tr_b16 v[60:61], v99 offset:0\n"
    "ds_read_b64_tr_b16 v[62:63], v99 offset:2048\n"
    "ds_read_b64_tr_b16 v[64:65], v96 offset:4096\n"
    "ds_read_b64_tr_b16 v[66:67], v96 offset:6144\n"
    "ds_read_b64_tr_b16 v[68:69], v97 offset:4096\n"
    "ds_read_b64_tr_b16 v[70:71], v97 offset:6144\n"
    "ds_read_b64_tr_b16 v[72:73], v98 offset:4096\n"
    "ds_read_b64_tr_b16 v[74:75], v98 offset:6144\n"
    "ds_read_b64_tr_b16 v[76:77], v99 offset:4096\n"
    "s_mov_b32 s79, 65536\n"
    ".Lo_poll:\n"
    "buffer_load_dword v3, v157, s[48:51], 0 offen sc1\n"
    "s_waitcnt vmcnt(0)\n"
    "v_cmp_ne_u32 vcc, s64, v3\n"
    "s_sub_u32 s79, s79, 1\n"
    "s_cbranch_vccz .Lo_done\n"
    "s_cbranch_scc0 .Lo_poll\n"
    ".Lo_done:\n"
    "buffer_load_dwordx4 a[0:3], v153, s[48:51], s14 offen offset:0 sc1\n"
    "buffer_load_dwordx4 a[4:7], v153, s[48:51], s15 offen offset:0 sc1\n"
    "buffer_load_dwordx4 a[8:11], v153, s[48:51], s14 offen offset:1024 sc1\n"
    "buffer_load_dwordx4 a[12:15], v153, s[48:51], s15 offen offset:1024 sc1\n"
    "buffer_load_dwordx4 a[16:19], v153, s[48:51], s14 offen offset:2048 sc1\n"
    "buffer_load_dwordx4 a[20:23], v153, s[48:51], s15 offen offset:2048 sc1\n"
    "buffer_load_dwordx4 a[24:27], v153, s[48:51], s14 offen offset:3072 sc1\n"
    "buffer_load_dwordx4 a[28:31], v153, s[48:51], s15 offen offset:3072 sc1\n"
    "s_add_u32 s14, s14, 4096\n"
    "buffer_load_dwordx4 a[32:35], v153, s[48:51], s14 offen offset:0 sc1\n"
    "s_add_u32 s15, s15, 4096\n"
    "buffer_load_dwordx4 a[36:39], v153, s[48:51], s15 offen offset:0 sc1\n"
    "buffer_load_dwordx4 a[40:43], v153, s[48:51], s14 offen offset:1024 sc1\n"
    "buffer_load_dwordx4 a[44:47], v153, s[48:51], s15 offen offset:1024 sc1\n"
    "buffer_load_dwordx4 a[48:51], v153, s[48:51], s14 offen offset:2048 sc1\n"
    "buffer_load_dwordx4 a[52:55], v153, s[48:51], s15 offen offset:2048 sc1\n"
    "buffer_load_dwordx4 a[56:59], v153, s[48:51], s14 offen offset:3072 sc1\n"
    "buffer_load_dwordx4 a[60:63], v153, s[48:51], s15 offen offset:3072 sc1\n"
    "ds_read_b64_tr_b16 v[78:79], v99 offset:6144\n"
    "s_waitcnt vmcnt(14) lgkmcnt(8)\n"
    "v_mfma_f32_16x16x32_bf16 v[16:19], v[48:51], a[0:3], 0\n"
    "ds_read_b64_tr_b16 v[80:81], v96 offset:16384\n"
    "ds_read_b64_tr_b16 v[82:83], v96 offset:18432\n"
    "v_mfma_f32_16x16x32_bf16 v[20:23], v[48:51], a[4:7], 0\n"
    "ds_read_b64_tr_b16 v[84:85], v97 offset:16384\n"
    "ds_read_b64_tr_b16 v[86:87], v97 offset:18432\n"
    "v_mfma_f32_16x16x32_bf16 v[24:27], v[52:55], a[0:3], 0\n"
    "ds_read_b64_tr_b16 v[88:89], v98 offset:16384\n"
    "v_mfma_f32_16x16x32_bf16 v[28:31], v[52:55], a[4:7], 0\n"
    "ds_read_b64_tr_b16 v[90:91], v98 offset:18432\n"
    "v_mfma_f32_16x16x32_bf16 v[32:35], v[56:59], a[0:3], 0\n"
    "ds_read_b64_tr_b16 v[92:93], v99 offset:16384\n"
    "v_mfma_f32_16x16x32_bf16 v[36:39], v[56:59], a[4:7], 0\n"
    "ds_read_b64_tr_b16 v[94:95], v99 offset:18432\n"
    "v_mfma_f32_16x16x32_bf16 v[40:43], v[60:63], a[0:3], 0\n"
    "s_add_u32 s14, s14, 4096\n"
    "buffer_load_dwordx4 a[64:67], v153, s[48:51], s14 offen offset:0 sc1\n"
    "v_mfma_f32_16x16x32_bf16 v[44:47], v[60:63], a[4:7], 0\n"
    "s_add_u32 s15, s15, 4096\n"
    "buffer_load_dwordx4 a[68:71], v153, s[48:51], s15 offen offset:0 sc1\n"
    "s_waitcnt vmcnt(14) lgkmcnt(8)\n"
    "v_mfma_f32_16x16x32_bf16 v[16:19], v[64:67], a[8:11], v[16:19]\n"
    "ds_read_b64_tr_b16 v[48:49], v96 offset:20480\n"
    "ds_read_b64_tr_b16 v[50:51], v96 offset:22528\n"
    "v_mfma_f32_16x16x32_bf16 v[20:23], v[64:67], a[12:15], v[20:23]\n"
    "ds_read_b64_tr_b16 v[52:53], v97 offset:20480\n"
    "ds_read_b64_tr_b16 v[54:55], v97 offset:22528\n"
    "v_mfma_f32_16x16x32_bf16 v[24:27], v[68:71], a[8:11], v[24:27]\n"
    "ds_read_b64_tr_b16 v[56:57], v98 offset:20480\n"
    "v_mfma_f32_16x16x32_bf16 v[28:31], v[68:71], a[12:15], v[28:31]\n"
    "ds_read_b64_tr_b16 v[58:59], v98 offset:22528\n"
    "v_mfma_f32_16x16x32_bf16 v[32:35], v[72:75], a[8:11], v[32:35]\n"
    "ds_read_b64_tr_b16 v[60:61], v99 offset:20480\n"
    "v_mfma_f32_16x16x32_bf16 v[36:39], v[72:75], a[12:15], v[36:39]\n"
    "ds_read_b64_tr_b16 v[62:63], v99 offset:22528\n"
    "v_mfma_f32_16x16x32_bf16 v[40:43], v[76:79], a[8:11], v[40:43]\n"
    "buffer_load_dwordx4 a[72:75], v153, s[48:51], s14 offen offset:1024 sc1\n"
    "v_mfma_f32_16x16x32_bf16 v[44:47], v[76:79], a[12:15], v[44:47]\n"
    "buffer_load_dwordx4 a[76:79], v153, s[48:51], s15 offen offset:1024 sc1\n"
    "s_waitcnt vmcnt(14) lgkmcnt(8)\n"
    "v_mfma_f32_16x16x32_bf16 v[16:19], v[80:83], a[16:19], v[16:19]\n"
    "ds_read_b64_tr_b16 v[64:65], v96 offset:32768\n"
    "ds_read_b64_tr_b16 v[66:67], v96 offset:34816\n"
    "v_mfma_f32_16x16x32_bf16 v[20:23], v[80:83], a[20:23], v[20:23]\n"
    "ds_read_b64_tr_b16 v[68:69], v97 offset:32768\n"
    "ds_read_b64_tr_b16 v[70:71], v97 offset:34816\n"
    "v_mfma_f32_16x16x32_bf16 v[24:27], v[84:87], a[16:19], v[24:27]\n"
    "ds_read_b64_tr_b16 v[72:73], v98 offset:32768\n"
    "v_mfma_f32_16x16x32_bf16 v[28:31], v[84:87], a[20:23], v[28:31]\n"
    "ds_read_b64_tr_b16 v[74:75], v98 offset:34816\n"
    "v_mfma_f32_16x16x32_bf16 v[32:35], v[88:91], a[16:19], v[32:35]\n"
    "ds_read_b64_tr_b16 v[76:77], v99 offset:32768\n"
    "v_mfma_f32_16x16x32_bf16 v[36:39], v[88:91], a[20:23], v[36:39]\n"
    "ds_read_b64_tr_b16 v[78:79], v99 offset:34816\n"
    "v_mfma_f32_16x16x32_bf16 v[40:43], v[92:95], a[16:19], v[40:43]\n"
    "buffer_load_dwordx4 a[80:83], v153, s[48:51], s14 offen offset:2048 sc1\n"
    "v_mfma_f32_16x16x32_bf16 v[44:47], v[92:95], a[20:23], v[44:47]\n"
    "buffer_load_dwordx4 a[84:87], v153, s[48:51], s15 offen offset:2048 sc1\n"
    "s_waitcnt vmcnt(14) lgkmcnt(8)\n"
    "v_mfma_f32_16x16x32_bf16 v[16:19], v[48:51], a[24:27], v[16:19]\n"
    "ds_read_b64_tr_b16 v[80:81], v96 offset:36864\n"
    "ds_read_b64_tr_b16 v[82:83], v96 offset:38912\n"
    "v_mfma_f32_16x16x32_bf16 v[20:23], v[48:51], a[28:31], v[20:23]\n"
    "ds_read_b64_tr_b16 v[84:85], v97 offset:36864\n"
    "ds_read_b64_tr_b16 v[86:87], v97 offset:38912\n"
    "v_mfma_f32_16x16x32_bf16 v[24:27], v[52:55], a[24:27], v[24:27]\n"
    "ds_read_b64_tr_b16 v[88:89], v98 offset:36864\n"
    "v_mfma_f32_16x16x32_bf16 v[28:31], v[52:55], a[28:31], v[28:31]\n"
    "ds_read_b64_tr_b16 v[90:91], v98 offset:38912\n"
    "v_mfma_f32_16x16x32_bf16 v[32:35], v[56:59], a[24:27], v[32:35]\n"
    "ds_read_b64_tr_b16 v[92:93], v99 offset:36864\n"
    "v_mfma_f32_16x16x32_bf16 v[36:39], v[56:59], a[28:31], v[36:39]\n"
    "ds_read_b64_tr_b16 v[94:95], v99 offset:38912\n"
    "v_mfma_f32_16x16x32_bf16 v[40:43], v[60:63], a[24:27], v[40:43]\n"
    "buffer_load_dwordx4 a[88:91], v153, s[48:51], s14 offen offset:3072 sc1\n"
    "v_mfma_f32_16x16x32_bf16 v[44:47], v[60:63], a[28:31], v[44:47]\n"
    "buffer_load_dwordx4 a[92:95], v153, s[48:51], s15 offen offset:3072 sc1\n"
    "s_waitcnt vmcnt(14) lgkmcnt(8)\n"
    "v_mfma_f32_16x16x32_bf16 v[16:19], v[64:67], a[32:35], v[16:19]\n"
    "ds_read_b64_tr_b16 v[48:49], v96 offset:49152\n"
    "ds_read_b64_tr_b16 v[50:51], v96 offset:51200\n"
    "v_mfma_f32_16x16x32_bf16 v[20:23], v[64:67], a[36:39], v[20:23]\n"
    "ds_read_b64_tr_b16 v[52:53], v97 offset:49152\n"
    "ds_read_b64_tr_b16 v[54:55], v97 offset:51200\n"
    "v_mfma_f32_16x16x32_bf16 v[24:27], v[68:71], a[32:35], v[24:27]\n"
    "ds_read_b64_tr_b16 v[56:57], v98 offset:49152\n"
    "v_mfma_f32_16x16x32_bf16 v[28:31], v[68:71], a[36:39], v[28:31]\n"
    "ds_read_b64_tr_b16 v[58:59], v98 offset:51200\n"
    "v_mfma_f32_16x16x32_bf16 v[32:35], v[72:75], a[32:35], v[32:35]\n"
    "ds_read_b64_tr_b16 v[60:61], v99 offset:49152\n"
    "v_mfma_f32_16x16x32_bf16 v[36:39], v[72:75], a[36:39], v[36:39]\n"
    "ds_read_b64_tr_b16 v[62:63], v99 offset:51200\n"
    "v_mfma_f32_16x16x32_bf16 v[40:43], v[76:79], a[32:35], v[40:43]\n"
    "s_add_u32 s14, s14, 4096\n"
    "buffer_load_dwordx4 a[96:99], v153, s[48:51], s14 offen offset:0 sc1\n"
    "v_mfma_f32_16x16x32_bf16 v[44:47], v[76:79], a[36:39], v[44:47]\n"
    "s_add_u32 s15, s15, 4096\n"
    "buffer_load_dwordx4 a[100:103], v153, s[48:51], s15 offen offset:0 sc1\n"
    "s_waitcnt vmcnt(14) lgkmcnt(8)\n"
    "v_mfma_f32_16x16x32_bf16 v[16:19], v[80:83], a[40:43], v[16:19]\n"
    "ds_read_b64_tr_b16 v[64:65], v96 offset:53248\n"
    "ds_read_b64_tr_b16 v[66:67], v96 offset:55296\n"
    "v_mfma_f32_16x16x32_bf16 v[20:23], v[80:83], a[44:47], v[20:23]\n"
    "ds_read_b64_tr_b16 v[68:69], v97 offset:53248\n"
    "ds_read_b64_tr_b16 v[70:71], v97 offset:55296\n"
    "v_mfma_f32_16x16x32_bf16 v[24:27], v[84:87], a[40:43], v[24:27]\n"
    "ds_read_b64_tr_b16 v[72:73], v98 offset:53248\n"
    "v_mfma_f32_16x16x32_bf16 v[28:31], v[84:87], a[44:47], v[28:31]\n"
    "ds_read_b64_tr_b16 v[74:75], v98 offset:55296\n"
    "v_mfma_f32_16x16x32_bf16 v[32:35], v[88:91], a[40:43], v[32:35]\n"
    "ds_read_b64_tr_b16 v[76:77], v99 offset:53248\n"
    "v_mfma_f32_16x16x32_bf16 v[36:39], v[88:91], a[44:47], v[36:39]\n"
    "ds_read_b64_tr_b16 v[78:79], v99 offset:55296\n"
    "v_mfma_f32_16x16x32_bf16 v[40:43], v[92:95], a[40:43], v[40:43]\n"
    "buffer_load_dwordx4 a[104:107], v153, s[48:51], s14 offen offset:1024 sc1\n"
    "v_mfma_f32_16x16x32_bf16 v[44:47], v[92:95], a[44:47], v[44:47]\n"
    "buffer_load_dwordx4 a[108:111], v153, s[48:51], s15 offen offset:1024 sc1\n"
    "s_waitcnt vmcnt(14) lgkmcnt(8)\n"
    "v_mfma_f32_16x16x32_bf16 v[16:19], v[48:51], a[48:51], v[16:19]\n"
    "ds_read_b64_tr_b16 v[80:81], v96 offset:8192\n"
    "ds_read_b64_tr_b16 v[82:83], v96 offset:10240\n"
    "v_mfma_f32_16x16x32_bf16 v[20:23], v[48:51], a[52:55], v[20:23]\n"
    "ds_read_b64_tr_b16 v[84:85], v97 offset:8192\n"
    "ds_read_b64_tr_b16 v[86:87], v97 offset:10240\n"
    "v_mfma_f32_16x16x32_bf16 v[24:27], v[52:55], a[48:51], v[24:27]\n"
    "ds_read_b64_tr_b16 v[88:89], v98 offset:8192\n"
    "v_mfma_f32_16x16x32_bf16 v[28:31], v[52:55], a[52:55], v[28:31]\n"
    "ds_read_b64_tr_b16 v[90:91], v98 offset:10240\n"
    "v_mfma_f32_16x16x32_bf16 v[32:35], v[56:59], a[48:51], v[32:35]\n"
    "ds_read_b64_tr_b16 v[92:93], v99 offset:8192\n"
    "v_mfma_f32_16x16x32_bf16 v[36:39], v[56:59], a[52:55], v[36:39]\n"
    "ds_read_b64_tr_b16 v[94:95], v99 offset:10240\n"
    "v_mfma_f32_16x16x32_bf16 v[40:43], v[60:63], a[48:51], v[40:43]\n"
    "buffer_load_dwordx4 a[112:115], v153, s[48:51], s14 offen offset:2048 sc1\n"
    "v_mfma_f32_16x16x32_bf16 v[44:47], v[60:63], a[52:55], v[44:47]\n"
    "buffer_load_dwordx4 a[116:119], v153, s[48:51], s15 offen offset:2048 sc1\n"
    "s_waitcnt vmcnt(14) lgkmcnt(8)\n"
    "v_mfma_f32_16x16x32_bf16 v[16:19], v[64:67], a[56:59], v[16:19]\n"
    "ds_read_b64_tr_b16 v[48:49], v96 offset:12288\n"
    "ds_read_b64_tr_b16 v[50:51], v96 offset:14336\n"
    "v_mfma_f32_16x16x32_bf16 v[20:23], v[64:67], a[60:63], v[20:23]\n"
    "ds_read_b64_tr_b16 v[52:53], v97 offset:12288\n"
    "ds_read_b64_tr_b16 v[54:55], v97 offset:14336\n"
    "v_mfma_f32_16x16x32_bf16 v[24:27], v[68:71], a[56:59], v[24:27]\n"
    "ds_read_b64_tr_b16 v[56:57], v98 offset:12288\n"
    "v_mfma_f32_16x16x32_bf16 v[28:31], v[68:71], a[60:63], v[28:31]\n"
    "ds_read_b64_tr_b16 v[58:59], v98 offset:14336\n"
    "v_mfma_f32_16x16x32_bf16 v[32:35], v[72:75], a[56:59], v[32:35]\n"
    "ds_read_b64_tr_b16 v[60:61], v99 offset:12288\n"
    "v_mfma_f32_16x16x32_bf16 v[36:39], v[72:75], a[60:63], v[36:39]\n"
    "ds_read_b64_tr_b16 v[62:63], v99 offset:14336\n"
    "v_mfma_f32_16x16x32_bf16 v[40:43], v[76:79], a[56:59], v[40:43]\n"
    "buffer_load_dwordx4 a[120:123], v153, s[48:51], s14 offen offset:3072 sc1\n"
    "v_mfma_f32_16x16x32_bf16 v[44:47], v[76:79], a[60:63], v[44:47]\n"
    "buffer_load_dwordx4 a[124:127], v153, s[48:51], s15 offen offset:3072 sc1\n"
    "s_waitcnt vmcnt(14) lgkmcnt(8)\n"
    "v_mfma_f32_16x16x32_bf16 v[16:19], v[80:83], a[64:67], v[16:19]\n"
    "ds_read_b64_tr_b16 v[64:65], v96 offset:24576\n"
    "ds_read_b64_tr_b16 v[66:67], v96 offset:26624\n"
    "v_mfma_f32_16x16x32_bf16 v[20:23], v[80:83], a[68:71], v[20:23]\n"
    "ds_read_b64_tr_b16 v[68:69], v97 offset:24576\n"
    "ds_read_b64_tr_b16 v[70:71], v97 offset:26624\n"
    "v_mfma_f32_16x16x32_bf16 v[24:27], v[84:87], a[64:67], v[24:27]\n"
    "ds_read_b64_tr_b16 v[72:73], v98 offset:24576\n"
    "v_mfma_f32_16x16x32_bf16 v[28:31], v[84:87], a[68:71], v[28:31]\n"
    "ds_read_b64_tr_b16 v[74:75], v98 offset:26624\n"
    "v_mfma_f32_16x16x32_bf16 v[32:35], v[88:91], a[64:67], v[32:35]\n"
    "ds_read_b64_tr_b16 v[76:77], v99 offset:24576\n"
    "v_mfma_f32_16x16x32_bf16 v[36:39], v[88:91], a[68:71], v[36:39]\n"
    "ds_read_b64_tr_b16 v[78:79], v99 offset:26624\n"
    "v_mfma_f32_16x16x32_bf16 v[40:43], v[92:95], a[64:67], v[40:43]\n"
    "s_add_u32 s14, s14, 4096\n"
    "buffer_load_dwordx4 a[128:131], v153, s[48:51], s14 offen offset:0 sc1\n"
    "v_mfma_f32_16x16x32_bf16 v[44:47], v[92:95], a[68:71], v[44:47]\n"
    "s_add_u32 s15, s15, 4096\n"
    "buffer_load_dwordx4 a[132:135], v153, s[48:51], s15 offen offset:0 sc1\n"
    "s_waitcnt vmcnt(14) lgkmcnt(8)\n"
    "v_mfma_f32_16x16x32_bf16 v[16:19], v[48:51], a[72:75], v[16:19]\n"
    "ds_read_b64_tr_b16 v[80:81], v96 offset:28672\n"
    "ds_read_b64_tr_b16 v[82:83], v96 offset:30720\n"
    "v_mfma_f32_16x16x32_bf16 v[20:23], v[48:51], a[76:79], v[20:23]\n"
    "ds_read_b64_tr_b16 v[84:85], v97 offset:28672\n"
    "ds_read_b64_tr_b16 v[86:87], v97 offset:30720\n"
    "v_mfma_f32_16x16x32_bf16 v[24:27], v[52:55], a[72:75], v[24:27]\n"
    "ds_read_b64_tr_b16 v[88:89], v98 offset:28672\n"
    "v_mfma_f32_16x16x32_bf16 v[28:31], v[52:55], a[76:79], v[28:31]\n"
    "ds_read_b64_tr_b16 v[90:91], v98 offset:30720\n"
    "v_mfma_f32_16x16x32_bf16 v[32:35], v[56:59], a[72:75], v[32:35]\n"
    "ds_read_b64_tr_b16 v[92:93], v99 offset:28672\n"
    "v_mfma_f32_16x16x32_bf16 v[36:39], v[56:59], a[76:79], v[36:39]\n"
    "ds_read_b64_tr_b16 v[94:95], v99 offset:30720\n"
    "v_mfma_f32_16x16x32_bf16 v[40:43], v[60:63], a[72:75], v[40:43]\n"
    "buffer_load_dwordx4 a[136:139], v153, s[48:51], s14 offen offset:1024 sc1\n"
    "v_mfma_f32_16x16x32_bf16 v[44:47], v[60:63], a[76:79], v[44:47]\n"
    "buffer_load_dwordx4 a[140:143], v153, s[48:51], s15 offen offset:1024 sc1\n"
    "s_waitcnt vmcnt(14) lgkmcnt(8)\n"
    "v_mfma_f32_16x16x32_bf16 v[16:19], v[64:67], a[80:83], v[16:19]\n"
    "ds_read_b64_tr_b16 v[48:49], v96 offset:40960\n"
    "ds_read_b64_tr_b16 v[50:51], v96 offset:43008\n"
    "v_mfma_f32_16x16x32_bf16 v[20:23], v[64:67], a[84:87], v[20:23]\n"
    "ds_read_b64_tr_b16 v[52:53], v97 offset:40960\n"
    "ds_read_b64_tr_b16 v[54:55], v97 offset:43008\n"
    "v_mfma_f32_16x16x32_bf16 v[24:27], v[68:71], a[80:83], v[24:27]\n"
    "ds_read_b64_tr_b16 v[56:57], v98 offset:40960\n"
    "v_mfma_f32_16x16x32_bf16 v[28:31], v[68:71], a[84:87], v[28:31]\n"
    "ds_read_b64_tr_b16 v[58:59], v98 offset:43008\n"
    "v_mfma_f32_16x16x32_bf16 v[32:35], v[72:75], a[80:83], v[32:35]\n"
    "ds_read_b64_tr_b16 v[60:61], v99 offset:40960\n"
    "v_mfma_f32_16x16x32_bf16 v[36:39], v[72:75], a[84:87], v[36:39]\n"
    "ds_read_b64_tr_b16 v[62:63], v99 offset:43008\n"
    "v_mfma_f32_16x16x32_bf16 v[40:43], v[76:79], a[80:83], v[40:43]\n"
    "buffer_load_dwordx4 a[144:147], v153, s[48:51], s14 offen offset:2048 sc1\n"
    "v_mfma_f32_16x16x32_bf16 v[44:47], v[76:79], a[84:87], v[44:47]\n"
    "buffer_load_dwordx4 a[148:151], v153, s[48:51], s15 offen offset:2048 sc1\n"
    "s_waitcnt vmcnt(14) lgkmcnt(8)\n"
    "v_mfma_f32_16x16x32_bf16 v[16:19], v[80:83], a[88:91], v[16:19]\n"
    "ds_read_b64_tr_b16 v[64:65], v96 offset:45056\n"
    "ds_read_b64_tr_b16 v[66:67], v96 offset:47104\n"
    "v_mfma_f32_16x16x32_bf16 v[20:23], v[80:83], a[92:95], v[20:23]\n"
    "ds_read_b64_tr_b16 v[68:69], v97 offset:45056\n"
    "ds_read_b64_tr_b16 v[70:71], v97 offset:47104\n"
    "v_mfma_f32_16x16x32_bf16 v[24:27], v[84:87], a[88:91], v[24:27]\n"
    "ds_read_b64_tr_b16 v[72:73], v98 offset:45056\n"
    "v_mfma_f32_16x16x32_bf16 v[28:31], v[84:87], a[92:95], v[28:31]\n"
    "ds_read_b64_tr_b16 v[74:75], v98 offset:47104\n"
    "v_mfma_f32_16x16x32_bf16 v[32:35], v[88:91], a[88:91], v[32:35]\n"
    "ds_read_b64_tr_b16 v[76:77], v99 offset:45056\n"
    "v_mfma_f32_16x16x32_bf16 v[36:39], v[88:91], a[92:95], v[36:39]\n"
    "ds_read_b64_tr_b16 v[78:79], v99 offset:47104\n"
    "v_mfma_f32_16x16x32_bf16 v[40:43], v[92:95], a[88:91], v[40:43]\n"
    "buffer_load_dwordx4 a[152:155], v153, s[48:51], s14 offen offset:3072 sc1\n"
    "v_mfma_f32_16x16x32_bf16 v[44:47], v[92:95], a[92:95], v[44:47]\n"
    "buffer_load_dwordx4 a[156:159], v153, s[48:51], s15 offen offset:3072 sc1\n"
    "s_waitcnt vmcnt(14) lgkmcnt(8)\n"
    "v_mfma_f32_16x16x32_bf16 v[16:19], v[48:51], a[96:99], v[16:19]\n"
    "ds_read_b64_tr_b16 v[80:81], v96 offset:57344\n"
    "ds_read_b64_tr_b16 v[82:83], v96 offset:59392\n"
    "v_mfma_f32_16x16x32_bf16 v[20:23], v[48:51], a[100:103], v[20:23]\n"
    "ds_read_b64_tr_b16 v[84:85], v97 offset:57344\n"
    "ds_read_b64_tr_b16 v[86:87], v97 offset:59392\n"
    "v_mfma_f32_16x16x32_bf16 v[24:27], v[52:55], a[96:99], v[24:27]\n"
    "ds_read_b64_tr_b16 v[88:89], v98 offset:57344\n"
    "v_mfma_f32_16x16x32_bf16 v[28:31], v[52:55], a[100:103], v[28:31]\n"
    "ds_read_b64_tr_b16 v[90:91], v98 offset:59392\n"
    "v_mfma_f32_16x16x32_bf16 v[32:35], v[56:59], a[96:99], v[32:35]\n"
    "ds_read_b64_tr_b16 v[92:93], v99 offset:57344\n"
    "v_mfma_f32_16x16x32_bf16 v[36:39], v[56:59], a[100:103], v[36:39]\n"
    "ds_read_b64_tr_b16 v[94:95], v99 offset:59392\n"
    "v_mfma_f32_16x16x32_bf16 v[40:43], v[60:63], a[96:99], v[40:43]\n"
    "s_add_u32 s14, s14, 4096\n"
    "buffer_load_dwordx4 a[160:163], v153, s[48:51], s14 offen offset:0 sc1\n"
    "v_mfma_f32_16x16x32_bf16 v[44:47], v[60:63], a[100:103], v[44:47]\n"
    "s_add_u32 s15, s15, 4096\n"
    "buffer_load_dwordx4 a[164:167], v153, s[48:51], s15 offen offset:0 sc1\n"
    "s_waitcnt vmcnt(14) lgkmcnt(8)\n"
    "v_mfma_f32_16x16x32_bf16 v[16:19], v[64:67], a[104:107], v[16:19]\n"
    "ds_read_b64_tr_b16 v[48:49], v96 offset:61440\n"
    "ds_read_b64_tr_b16 v[50:51], v96 offset:63488\n"
    "v_mfma_f32_16x16x32_bf16 v[20:23], v[64:67], a[108:111], v[20:23]\n"
    "ds_read_b64_tr_b16 v[52:53], v97 offset:61440\n"
    "ds_read_b64_tr_b16 v[54:55], v97 offset:63488\n"
    "v_mfma_f32_16x16x32_bf16 v[24:27], v[68:71], a[104:107], v[24:27]\n"
    "ds_read_b64_tr_b16 v[56:57], v98 offset:61440\n"
    "v_mfma_f32_16x16x32_bf16 v[28:31], v[68:71], a[108:111], v[28:31]\n"
    "ds_read_b64_tr_b16 v[58:59], v98 offset:63488\n"
    "v_mfma_f32_16x16x32_bf16 v[32:35], v[72:75], a[104:107], v[32:35]\n"
    "ds_read_b64_tr_b16 v[60:61], v99 offset:61440\n"
    "v_mfma_f32_16x16x32_bf16 v[36:39], v[72:75], a[108:111], v[36:39]\n"
    "ds_read_b64_tr_b16 v[62:63], v99 offset:63488\n"
    "v_mfma_f32_16x16x32_bf16 v[40:43], v[76:79], a[104:107], v[40:43]\n"
    "buffer_load_dwordx4 a[168:171], v153, s[48:51], s14 offen offset:1024 sc1\n"
    "v_mfma_f32_16x16x32_bf16 v[44:47], v[76:79], a[108:111], v[44:47]\n"
    "buffer_load_dwordx4 a[172:175], v153, s[48:51], s15 offen offset:1024 sc1\n"
    "s_waitcnt vmcnt(14) lgkmcnt(8)\n"
    "v_mfma_f32_16x16x32_bf16 v[16:19], v[80:83], a[112:115], v[16:19]\n"
    "ds_read_b64_tr_b16 v[64:65], v100 offset:0\n"
    "ds_read_b64_tr_b16 v[66:67], v100 offset:2048\n"
    "v_mfma_f32_16x16x32_bf16 v[20:23], v[80:83], a[116:119], v[20:23]\n"
    "ds_read_b64_tr_b16 v[68:69], v101 offset:0\n"
    "ds_read_b64_tr_b16 v[70:71], v101 offset:2048\n"
    "v_mfma_f32_16x16x32_bf16 v[24:27], v[84:87], a[112:115], v[24:27]\n"
    "ds_read_b64_tr_b16 v[72:73], v102 offset:0\n"
    "v_mfma_f32_16x16x32_bf16 v[28:31], v[84:87], a[116:119], v[28:31]\n"
    "ds_read_b64_tr_b16 v[74:75], v102 offset:2048\n"
    "v_mfma_f32_16x16x32_bf16 v[32:35], v[88:91], a[112:115], v[32:35]\n"
    "ds_read_b64_tr_b16 v[76:77], v103 offset:0\n"
    "v_mfma_f32_16x16x32_bf16 v[36:39], v[88:91], a[116:119], v[36:39]\n"
    "ds_read_b64_tr_b16 v[78:79], v103 offset:2048\n"
    "v_mfma_f32_16x16x32_bf16 v[40:43], v[92:95], a[112:115], v[40:43]\n"
    "buffer_load_dwordx4 a[176:179], v153, s[48:51], s14 offen offset:2048 sc1\n"
    "v_mfma_f32_16x16x32_bf16 v[44:47], v[92:95], a[116:119], v[44:47]\n"
    "buffer_load_dwordx4 a[180:183], v153, s[48:51], s15 offen offset:2048 sc1\n"
    "s_waitcnt vmcnt(14) lgkmcnt(8)\n"
    "v_mfma_f32_16x16x32_bf16 v[16:19], v[48:51], a[120:123], v[16:19]\n"
    "ds_read_b64_tr_b16 v[80:81], v100 offset:4096\n"
    "ds_read_b64_tr_b16 v[82:83], v100 offset:6144\n"
    "v_mfma_f32_16x16x32_bf16 v[20:23], v[48:51], a[124:127], v[20:23]\n"
    "ds_read_b64_tr_b16 v[84:85], v101 offset:4096\n"
    "ds_read_b64_tr_b16 v[86:87], v101 offset:6144\n"
    "v_mfma_f32_16x16x32_bf16 v[24:27], v[52:55], a[120:123], v[24:27]\n"
    "ds_read_b64_tr_b16 v[88:89], v102 offset:4096\n"
    "v_mfma_f32_16x16x32_bf16 v[28:31], v[52:55], a[124:127], v[28:31]\n"
    "ds_read_b64_tr_b16 v[90:91], v102 offset:6144\n"
    "v_mfma_f32_16x16x32_bf16 v[32:35], v[56:59], a[120:123], v[32:35]\n"
    "ds_read_b64_tr_b16 v[92:93], v103 offset:4096\n"
    "v_mfma_f32_16x16x32_bf16 v[36:39], v[56:59], a[124:127], v[36:39]\n"
    "ds_read_b64_tr_b16 v[94:95], v103 offset:6144\n"
    "v_mfma_f32_16x16x32_bf16 v[40:43], v[60:63], a[120:123], v[40:43]\n"
    "buffer_load_dwordx4 a[184:187], v153, s[48:51], s14 offen offset:3072 sc1\n"
    "v_mfma_f32_16x16x32_bf16 v[44:47], v[60:63], a[124:127], v[44:47]\n"
    "buffer_load_dwordx4 a[188:191], v153, s[48:51], s15 offen offset:3072 sc1\n"
    "s_waitcnt vmcnt(14) lgkmcnt(8)\n"
    "v_mfma_f32_16x16x32_bf16 v[16:19], v[64:67], a[128:131], v[16:19]\n"
    "ds_read_b64_tr_b16 v[48:49], v100 offset:16384\n"
    "ds_read_b64_tr_b16 v[50:51], v100 offset:18432\n"
    "v_mfma_f32_16x16x32_bf16 v[20:23], v[64:67], a[132:135], v[20:23]\n"
    "ds_read_b64_tr_b16 v[52:53], v101 offset:16384\n"
    "ds_read_b64_tr_b16 v[54:55], v101 offset:18432\n"
    "v_mfma_f32_16x16x32_bf16 v[24:27], v[68:71], a[128:131], v[24:27]\n"
    "ds_read_b64_tr_b16 v[56:57], v102 offset:16384\n"
    "v_mfma_f32_16x16x32_bf16 v[28:31], v[68:71], a[132:135], v[28:31]\n"
    "ds_read_b64_tr_b16 v[58:59], v102 offset:18432\n"
    "v_mfma_f32_16x16x32_bf16 v[32:35], v[72:75], a[128:131], v[32:35]\n"
    "ds_read_b64_tr_b16 v[60:61], v103 offset:16384\n"
    "v_mfma_f32_16x16x32_bf16 v[36:39], v[72:75], a[132:135], v[36:39]\n"
    "ds_read_b64_tr_b16 v[62:63], v103 offset:18432\n"
    "v_mfma_f32_16x16x32_bf16 v[40:43], v[76:79], a[128:131], v[40:43]\n"
    "s_add_u32 s14, s14, 4096\n"
    "buffer_load_dwordx4 a[192:195], v153, s[48:51], s14 offen offset:0 sc1\n"
    "v_mfma_f32_16x16x32_bf16 v[44:47], v[76:79], a[132:135], v[44:47]\n"
    "s_add_u32 s15, s15, 4096\n"
    "buffer_load_dwordx4 a[196:199], v153, s[48:51], s15 offen offset:0 sc1\n"
    "s_waitcnt vmcnt(14) lgkmcnt(8)\n"
    "v_mfma_f32_16x16x32_bf16 v[16:19], v[80:83], a[136:139], v[16:19]\n"
    "ds_read_b64_tr_b16 v[64:65], v100 offset:20480\n"
    "ds_read_b64_tr_b16 v[66:67], v100 offset:22528\n"
    "v_mfma_f32_16x16x32_bf16 v[20:23], v[80:83], a[140:143], v[20:23]\n"
    "ds_read_b64_tr_b16 v[68:69], v101 offset:20480\n"
    "ds_read_b64_tr_b16 v[70:71], v101 offset:22528\n"
    "v_mfma_f32_16x16x32_bf16 v[24:27], v[84:87], a[136:139], v[24:27]\n"
    "ds_read_b64_tr_b16 v[72:73], v102 offset:20480\n"
    "v_mfma_f32_16x16x32_bf16 v[28:31], v[84:87], a[140:143], v[28:31]\n"
    "ds_read_b64_tr_b16 v[74:75], v102 offset:22528\n"
    "v_mfma_f32_16x16x32_bf16 v[32:35], v[88:91], a[136:139], v[32:35]\n"
    "ds_read_b64_tr_b16 v[76:77], v103 offset:20480\n"
    "v_mfma_f32_16x16x32_bf16 v[36:39], v[88:91], a[140:143], v[36:39]\n"
    "ds_read_b64_tr_b16 v[78:79], v103 offset:22528\n"
    "v_mfma_f32_16x16x32_bf16 v[40:43], v[92:95], a[136:139], v[40:43]\n"
    "buffer_load_dwordx4 a[200:203], v153, s[48:51], s14 offen offset:1024 sc1\n"
    "v_mfma_f32_16x16x32_bf16 v[44:47], v[92:95], a[140:143], v[44:47]\n"
    "buffer_load_dwordx4 a[204:207], v153, s[48:51], s15 offen offset:1024 sc1\n"
    "s_waitcnt vmcnt(14) lgkmcnt(8)\n"
    "v_mfma_f32_16x16x32_bf16 v[16:19], v[48:51], a[144:147], v[16:19]\n"
    "ds_read_b64_tr_b16 v[80:81], v100 offset:32768\n"
    "ds_read_b64_tr_b16 v[82:83], v100 offset:34816\n"
    "v_mfma_f32_16x16x32_bf16 v[20:23], v[48:51], a[148:151], v[20:23]\n"
    "ds_read_b64_tr_b16 v[84:85], v101 offset:32768\n"
    "ds_read_b64_tr_b16 v[86:87], v101 offset:34816\n"
    "v_mfma_f32_16x16x32_bf16 v[24:27], v[52:55], a[144:147], v[24:27]\n"
    "ds_read_b64_tr_b16 v[88:89], v102 offset:32768\n"
    "v_mfma_f32_16x16x32_bf16 v[28:31], v[52:55], a[148:151], v[28:31]\n"
    "ds_read_b64_tr_b16 v[90:91], v102 offset:34816\n"
    "v_mfma_f32_16x16x32_bf16 v[32:35], v[56:59], a[144:147], v[32:35]\n"
    "ds_read_b64_tr_b16 v[92:93], v103 offset:32768\n"
    "v_mfma_f32_16x16x32_bf16 v[36:39], v[56:59], a[148:151], v[36:39]\n"
    "ds_read_b64_tr_b16 v[94:95], v103 offset:34816\n"
    "v_mfma_f32_16x16x32_bf16 v[40:43], v[60:63], a[144:147], v[40:43]\n"
    "buffer_load_dwordx4 a[208:211], v153, s[48:51], s14 offen offset:2048 sc1\n"
    "v_mfma_f32_16x16x32_bf16 v[44:47], v[60:63], a[148:151], v[44:47]\n"
    "buffer_load_dwordx4 a[212:215], v153, s[48:51], s15 offen offset:2048 sc1\n"
    "s_waitcnt vmcnt(14) lgkmcnt(8)\n"
    "v_mfma_f32_16x16x32_bf16 v[16:19], v[64:67], a[152:155], v[16:19]\n"
    "ds_read_b64_tr_b16 v[48:49], v100 offset:36864\n"
    "ds_read_b64_tr_b16 v[50:51], v100 offset:38912\n"
    "v_mfma_f32_16x16x32_bf16 v[20:23], v[64:67], a[156:159], v[20:23]\n"
    "ds_read_b64_tr_b16 v[52:53], v101 offset:36864\n"
    "ds_read_b64_tr_b16 v[54:55], v101 offset:38912\n"
    "v_mfma_f32_16x16x32_bf16 v[24:27], v[68:71], a[152:155], v[24:27]\n"
    "ds_read_b64_tr_b16 v[56:57], v102 offset:36864\n"
    "v_mfma_f32_16x16x32_bf16 v[28:31], v[68:71], a[156:159], v[28:31]\n"
    "ds_read_b64_tr_b16 v[58:59], v102 offset:38912\n"
    "v_mfma_f32_16x16x32_bf16 v[32:35], v[72:75], a[152:155], v[32:35]\n"
    "ds_read_b64_tr_b16 v[60:61], v103 offset:36864\n"
    "v_mfma_f32_16x16x32_bf16 v[36:39], v[72:75], a[156:159], v[36:39]\n"
    "ds_read_b64_tr_b16 v[62:63], v103 offset:38912\n"
    "v_mfma_f32_16x16x32_bf16 v[40:43], v[76:79], a[152:155], v[40:43]\n"
    "buffer_load_dwordx4 a[216:219], v153, s[48:51], s14 offen offset:3072 sc1\n"
    "v_mfma_f32_16x16x32_bf16 v[44:47], v[76:79], a[156:159], v[44:47]\n"
    "buffer_load_dwordx4 a[220:223], v153, s[48:51], s15 offen offset:3072 sc1\n"
    "s_waitcnt vmcnt(14) lgkmcnt(8)\n"
    "v_mfma_f32_16x16x32_bf16 v[16:19], v[80:83], a[160:163], v[16:19]\n"
    "ds_read_b64_tr_b16 v[64:65], v100 offset:49152\n"
    "ds_read_b64_tr_b16 v[66:67], v100 offset:51200\n"
    "v_mfma_f32_16x16x32_bf16 v[20:23], v[80:83], a[164:167], v[20:23]\n"
    "ds_read_b64_tr_b16 v[68:69], v101 offset:49152\n"
    "ds_read_b64_tr_b16 v[70:71], v101 offset:51200\n"
    "v_mfma_f32_16x16x32_bf16 v[24:27], v[84:87], a[160:163], v[24:27]\n"
    "ds_read_b64_tr_b16 v[72:73], v102 offset:49152\n"
    "v_mfma_f32_16x16x32_bf16 v[28:31], v[84:87], a[164:167], v[28:31]\n"
    "ds_read_b64_tr_b16 v[74:75], v102 offset:51200\n"
    "v_mfma_f32_16x16x32_bf16 v[32:35], v[88:91], a[160:163], v[32:35]\n"
    "ds_read_b64_tr_b16 v[76:77], v103 offset:49152\n"
    "v_mfma_f32_16x16x32_bf16 v[36:39], v[88:91], a[164:167], v[36:39]\n"
    "ds_read_b64_tr_b16 v[78:79], v103 offset:51200\n"
    "v_mfma_f32_16x16x32_bf16 v[40:43], v[92:95], a[160:163], v[40:43]\n"
    "s_add_u32 s14, s14, 4096\n"
    "buffer_load_dwordx4 a[224:227], v153, s[48:51], s14 offen offset:0 sc1\n"
    "v_mfma_f32_16x16x32_bf16 v[44:47], v[92:95], a[164:167], v[44:47]\n"
    "s_add_u32 s15, s15, 4096\n"
    "buffer_load_dwordx4 a[228:231], v153, s[48:51], s15 offen offset:0 sc1\n"
    "s_waitcnt vmcnt(14) lgkmcnt(8)\n"
    "v_mfma_f32_16x16x32_bf16 v[16:19], v[48:51], a[168:171], v[16:19]\n"
    "ds_read_b64_tr_b16 v[80:81], v100 offset:53248\n"
    "ds_read_b64_tr_b16 v[82:83], v100 offset:55296\n"
    "v_mfma_f32_16x16x32_bf16 v[20:23], v[48:51], a[172:175], v[20:23]\n"
    "ds_read_b64_tr_b16 v[84:85], v101 offset:53248\n"
    "ds_read_b64_tr_b16 v[86:87], v101 offset:55296\n"
    "v_mfma_f32_16x16x32_bf16 v[24:27], v[52:55], a[168:171], v[24:27]\n"
    "ds_read_b64_tr_b16 v[88:89], v102 offset:53248\n"
    "v_mfma_f32_16x16x32_bf16 v[28:31], v[52:55], a[172:175], v[28:31]\n"
    "ds_read_b64_tr_b16 v[90:91], v102 offset:55296\n"
    "v_mfma_f32_16x16x32_bf16 v[32:35], v[56:59], a[168:171], v[32:35]\n"
    "ds_read_b64_tr_b16 v[92:93], v103 offset:53248\n"
    "v_mfma_f32_16x16x32_bf16 v[36:39], v[56:59], a[172:175], v[36:39]\n"
    "ds_read_b64_tr_b16 v[94:95], v103 offset:55296\n"
    "v_mfma_f32_16x16x32_bf16 v[40:43], v[60:63], a[168:171], v[40:43]\n"
    "buffer_load_dwordx4 a[232:235], v153, s[48:51], s14 offen offset:1024 sc1\n"
    "v_mfma_f32_16x16x32_bf16 v[44:47], v[60:63], a[172:175], v[44:47]\n"
    "buffer_load_dwordx4 a[236:239], v153, s[48:51], s15 offen offset:1024 sc1\n"
    "s_waitcnt vmcnt(14) lgkmcnt(8)\n"
    "v_mfma_f32_16x16x32_bf16 v[16:19], v[64:67], a[176:179], v[16:19]\n"
    "ds_read_b64_tr_b16 v[48:49], v100 offset:8192\n"
    "ds_read_b64_tr_b16 v[50:51], v100 offset:10240\n"
    "v_mfma_f32_16x16x32_bf16 v[20:23], v[64:67], a[180:183], v[20:23]\n"
    "ds_read_b64_tr_b16 v[52:53], v101 offset:8192\n"
    "ds_read_b64_tr_b16 v[54:55], v101 offset:10240\n"
    "v_mfma_f32_16x16x32_bf16 v[24:27], v[68:71], a[176:179], v[24:27]\n"
    "ds_read_b64_tr_b16 v[56:57], v102 offset:8192\n"
    "v_mfma_f32_16x16x32_bf16 v[28:31], v[68:71], a[180:183], v[28:31]\n"
    "ds_read_b64_tr_b16 v[58:59], v102 offset:10240\n"
    "v_mfma_f32_16x16x32_bf16 v[32:35], v[72:75], a[176:179], v[32:35]\n"
    "ds_read_b64_tr_b16 v[60:61], v103 offset:8192\n"
    "v_mfma_f32_16x16x32_bf16 v[36:39], v[72:75], a[180:183], v[36:39]\n"
    "ds_read_b64_tr_b16 v[62:63], v103 offset:10240\n"
    "v_mfma_f32_16x16x32_bf16 v[40:43], v[76:79], a[176:179], v[40:43]\n"
    "buffer_load_dwordx4 a[240:243], v153, s[48:51], s14 offen offset:2048 sc1\n"
    "v_mfma_f32_16x16x32_bf16 v[44:47], v[76:79], a[180:183], v[44:47]\n"
    "buffer_load_dwordx4 a[244:247], v153, s[48:51], s15 offen offset:2048 sc1\n"
    "s_waitcnt vmcnt(14) lgkmcnt(8)\n"
    "v_mfma_f32_16x16x32_bf16 v[16:19], v[80:83], a[184:187], v[16:19]\n"
    "ds_read_b64_tr_b16 v[64:65], v100 offset:12288\n"
    "ds_read_b64_tr_b16 v[66:67], v100 offset:14336\n"
    "v_mfma_f32_16x16x32_bf16 v[20:23], v[80:83], a[188:191], v[20:23]\n"
    "ds_read_b64_tr_b16 v[68:69], v101 offset:12288\n"
    "ds_read_b64_tr_b16 v[70:71], v101 offset:14336\n"
    "v_mfma_f32_16x16x32_bf16 v[24:27], v[84:87], a[184:187], v[24:27]\n"
    "ds_read_b64_tr_b16 v[72:73], v102 offset:12288\n"
    "v_mfma_f32_16x16x32_bf16 v[28:31], v[84:87], a[188:191], v[28:31]\n"
    "ds_read_b64_tr_b16 v[74:75], v102 offset:14336\n"
    "v_mfma_f32_16x16x32_bf16 v[32:35], v[88:91], a[184:187], v[32:35]\n"
    "ds_read_b64_tr_b16 v[76:77], v103 offset:12288\n"
    "v_mfma_f32_16x16x32_bf16 v[36:39], v[88:91], a[188:191], v[36:39]\n"
    "ds_read_b64_tr_b16 v[78:79], v103 offset:14336\n"
    "v_mfma_f32_16x16x32_bf16 v[40:43], v[92:95], a[184:187], v[40:43]\n"
    "buffer_load_dwordx4 a[248:251], v153, s[48:51], s14 offen offset:3072 sc1\n"
    "v_mfma_f32_16x16x32_bf16 v[44:47], v[92:95], a[188:191], v[44:47]\n"
    "buffer_load_dwordx4 a[252:255], v153, s[48:51], s15 offen offset:3072 sc1\n"
    "s_waitcnt vmcnt(14) lgkmcnt(8)\n"
    "v_mfma_f32_16x16x32_bf16 v[16:19], v[48:51], a[192:195], v[16:19]\n"
    "ds_read_b64_tr_b16 v[80:81], v100 offset:24576\n"
    "ds_read_b64_tr_b16 v[82:83], v100 offset:26624\n"
    "v_mfma_f32_16x16x32_bf16 v[20:23], v[48:51], a[196:199], v[20:23]\n"
    "ds_read_b64_tr_b16 v[84:85], v101 offset:24576\n"
    "ds_read_b64_tr_b16 v[86:87], v101 offset:26624\n"
    "v_mfma_f32_16x16x32_bf16 v[24:27], v[52:55], a[192:195], v[24:27]\n"
    "ds_read_b64_tr_b16 v[88:89], v102 offset:24576\n"
    "v_mfma_f32_16x16x32_bf16 v[28:31], v[52:55], a[196:199], v[28:31]\n"
    "ds_read_b64_tr_b16 v[90:91], v102 offset:26624\n"
    "v_mfma_f32_16x16x32_bf16 v[32:35], v[56:59], a[192:195], v[32:35]\n"
    "ds_read_b64_tr_b16 v[92:93], v103 offset:24576\n"
    "v_mfma_f32_16x16x32_bf16 v[36:39], v[56:59], a[196:199], v[36:39]\n"
    "ds_read_b64_tr_b16 v[94:95], v103 offset:26624\n"
    "v_mfma_f32_16x16x32_bf16 v[40:43], v[60:63], a[192:195], v[40:43]\n"
    "v_mfma_f32_16x16x32_bf16 v[44:47], v[60:63], a[196:199], v[44:47]\n"
    "s_waitcnt vmcnt(12) lgkmcnt(8)\n"
    "v_mfma_f32_16x16x32_bf16 v[16:19], v[64:67], a[200:203], v[16:19]\n"
    "ds_read_b64_tr_b16 v[48:49], v100 offset:28672\n"
    "ds_read_b64_tr_b16 v[50:51], v100 offset:30720\n"
    "v_mfma_f32_16x16x32_bf16 v[20:23], v[64:67], a[204:207], v[20:23]\n"
    "ds_read_b64_tr_b16 v[52:53], v101 offset:28672\n"
    "ds_read_b64_tr_b16 v[54:55], v101 offset:30720\n"
    "v_mfma_f32_16x16x32_bf16 v[24:27], v[68:71], a[200:203], v[24:27]\n"
    "ds_read_b64_tr_b16 v[56:57], v102 offset:28672\n"
    "v_mfma_f32_16x16x32_bf16 v[28:31], v[68:71], a[204:207], v[28:31]\n"
    "ds_read_b64_tr_b16 v[58:59], v102 offset:30720\n"
    "v_mfma_f32_16x16x32_bf16 v[32:35], v[72:75], a[200:203], v[32:35]\n"
    "ds_read_b64_tr_b16 v[60:61], v103 offset:28672\n"
    "v_mfma_f32_16x16x32_bf16 v[36:39], v[72:75], a[204:207], v[36:39]\n"
    "ds_read_b64_tr_b16 v[62:63], v103 offset:30720\n"
    "v_mfma_f32_16x16x32_bf16 v[40:43], v[76:79], a[200:203], v[40:43]\n"
    "v_mfma_f32_16x16x32_bf16 v[44:47], v[76:79], a[204:207], v[44:47]\n"
    "s_waitcnt vmcnt(10) lgkmcnt(8)\n"
    "v_mfma_f32_16x16x32_bf16 v[16:19], v[80:83], a[208:211], v[16:19]\n"
    "ds_read_b64_tr_b16 v[64:65], v100 offset:40960\n"
    "ds_read_b64_tr_b16 v[66:67], v100 offset:43008\n"
    "v_mfma_f32_16x16x32_bf16 v[20:23], v[80:83], a[212:215], v[20:23]\n"
    "ds_read_b64_tr_b16 v[68:69], v101 offset:40960\n"
    "ds_read_b64_tr_b16 v[70:71], v101 offset:43008\n"
    "v_mfma_f32_16x16x32_bf16 v[24:27], v[84:87], a[208:211], v[24:27]\n"
    "ds_read_b64_tr_b16 v[72:73], v102 offset:40960\n"
    "v_mfma_f32_16x16x32_bf16 v[28:31], v[84:87], a[212:215], v[28:31]\n"
    "ds_read_b64_tr_b16 v[74:75], v102 offset:43008\n"
    "v_mfma_f32_16x16x32_bf16 v[32:35], v[88:91], a[208:211], v[32:35]\n"
    "ds_read_b64_tr_b16 v[76:77], v103 offset:40960\n"
    "v_mfma_f32_16x16x32_bf16 v[36:39], v[88:91], a[212:215], v[36:39]\n"
    "ds_read_b64_tr_b16 v[78:79], v103 offset:43008\n"
    "v_mfma_f32_16x16x32_bf16 v[40:43], v[92:95], a[208:211], v[40:43]\n"
    "v_mfma_f32_16x16x32_bf16 v[44:47], v[92:95], a[212:215], v[44:47]\n"
    "s_waitcnt vmcnt(8) lgkmcnt(8)\n"
    "v_mfma_f32_16x16x32_bf16 v[16:19], v[48:51], a[216:219], v[16:19]\n"
    "ds_read_b64_tr_b16 v[80:81], v100 offset:45056\n"
    "ds_read_b64_tr_b16 v[82:83], v100 offset:47104\n"
    "v_mfma_f32_16x16x32_bf16 v[20:23], v[48:51], a[220:223], v[20:23]\n"
    "ds_read_b64_tr_b16 v[84:85], v101 offset:45056\n"
    "ds_read_b64_tr_b16 v[86:87], v101 offset:47104\n"
    "v_mfma_f32_16x16x32_bf16 v[24:27], v[52:55], a[216:219], v[24:27]\n"
    "ds_read_b64_tr_b16 v[88:89], v102 offset:45056\n"
    "v_mfma_f32_16x16x32_bf16 v[28:31], v[52:55], a[220:223], v[28:31]\n"
    "ds_read_b64_tr_b16 v[90:91], v102 offset:47104\n"
    "v_mfma_f32_16x16x32_bf16 v[32:35], v[56:59], a[216:219], v[32:35]\n"
    "ds_read_b64_tr_b16 v[92:93], v103 offset:45056\n"
    "v_mfma_f32_16x16x32_bf16 v[36:39], v[56:59], a[220:223], v[36:39]\n"
    "ds_read_b64_tr_b16 v[94:95], v103 offset:47104\n"
    "v_mfma_f32_16x16x32_bf16 v[40:43], v[60:63], a[216:219], v[40:43]\n"
    "v_mfma_f32_16x16x32_bf16 v[44:47], v[60:63], a[220:223], v[44:47]\n"
    "s_waitcnt vmcnt(6) lgkmcnt(8)\n"
    "v_mfma_f32_16x16x32_bf16 v[16:19], v[64:67], a[224:227], v[16:19]\n"
    "ds_read_b64_tr_b16 v[48:49], v100 offset:57344\n"
    "ds_read_b64_tr_b16 v[50:51], v100 offset:59392\n"
    "v_mfma_f32_16x16x32_bf16 v[20:23], v[64:67], a[228:231], v[20:23]\n"
    "ds_read_b64_tr_b16 v[52:53], v101 offset:57344\n"
    "ds_read_b64_tr_b16 v[54:55], v101 offset:59392\n"
    "v_mfma_f32_16x16x32_bf16 v[24:27], v[68:71], a[224:227], v[24:27]\n"
    "ds_read_b64_tr_b16 v[56:57], v102 offset:57344\n"
    "v_mfma_f32_16x16x32_bf16 v[28:31], v[68:71], a[228:231], v[28:31]\n"
    "ds_read_b64_tr_b16 v[58:59], v102 offset:59392\n"
    "v_mfma_f32_16x16x32_bf16 v[32:35], v[72:75], a[224:227], v[32:35]\n"
    "ds_read_b64_tr_b16 v[60:61], v103 offset:57344\n"
    "v_mfma_f32_16x16x32_bf16 v[36:39], v[72:75], a[228:231], v[36:39]\n"
    "ds_read_b64_tr_b16 v[62:63], v103 offset:59392\n"
    "v_mfma_f32_16x16x32_bf16 v[40:43], v[76:79], a[224:227], v[40:43]\n"
    "v_mfma_f32_16x16x32_bf16 v[44:47], v[76:79], a[228:231], v[44:47]\n"
    "s_waitcnt vmcnt(4) lgkmcnt(8)\n"
    "v_mfma_f32_16x16x32_bf16 v[16:19], v[80:83], a[232:235], v[16:19]\n"
    "ds_read_b64_tr_b16 v[64:65], v100 offset:61440\n"
    "ds_read_b64_tr_b16 v[66:67], v100 offset:63488\n"
    "v_mfma_f32_16x16x32_bf16 v[20:23], v[80:83], a[236:239], v[20:23]\n"
    "ds_read_b64_tr_b16 v[68:69], v101 offset:61440\n"
    "ds_read_b64_tr_b16 v[70:71], v101 offset:63488\n"
    "v_mfma_f32_16x16x32_bf16 v[24:27], v[84:87], a[232:235], v[24:27]\n"
    "ds_read_b64_tr_b16 v[72:73], v102 offset:61440\n"
    "v_mfma_f32_16x16x32_bf16 v[28:31], v[84:87], a[236:239], v[28:31]\n"
    "ds_read_b64_tr_b16 v[74:75], v102 offset:63488\n"
    "v_mfma_f32_16x16x32_bf16 v[32:35], v[88:91], a[232:235], v[32:35]\n"
    "ds_read_b64_tr_b16 v[76:77], v103 offset:61440\n"
    "v_mfma_f32_16x16x32_bf16 v[36:39], v[88:91], a[236:239], v[36:39]\n"
    "ds_read_b64_tr_b16 v[78:79], v103 offset:63488\n"
    "v_mfma_f32_16x16x32_bf16 v[40:43], v[92:95], a[232:235], v[40:43]\n"
    "v_mfma_f32_16x16x32_bf16 v[44:47], v[92:95], a[236:239], v[44:47]\n"
    "s_waitcnt vmcnt(2) lgkmcnt(8)\n"
    "v_mfma_f32_16x16x32_bf16 v[16:19], v[48:51], a[240:243], v[16:19]\n"
    "v_mfma_f32_16x16x32_bf16 v[20:23], v[48:51], a[244:247], v[20:23]\n"
    "v_mfma_f32_16x16x32_bf16 v[24:27], v[52:55], a[240:243], v[24:27]\n"
    "v_mfma_f32_16x16x32_bf16 v[28:31], v[52:55], a[244:247], v[28:31]\n"
    "v_mfma_f32_16x16x32_bf16 v[32:35], v[56:59], a[240:243], v[32:35]\n"
    "v_mfma_f32_16x16x32_bf16 v[36:39], v[56:59], a[244:247], v[36:39]\n"
    "v_mfma_f32_16x16x32_bf16 v[40:43], v[60:63], a[240:243], v[40:43]\n"
    "v_mfma_f32_16x16x32_bf16 v[44:47], v[60:63], a[244:247], v[44:47]\n"
    "s_waitcnt vmcnt(0) lgkmcnt(0)\n"
    "v_mfma_f32_16x16x32_bf16 v[16:19], v[64:67], a[248:251], v[16:19]\n"
    "v_mfma_f32_16x16x32_bf16 v[20:23], v[64:67], a[252:255], v[20:23]\n"
    "v_mfma_f32_16x16x32_bf16 v[24:27], v[68:71], a[248:251], v[24:27]\n"
    "v_mfma_f32_16x16x32_bf16 v[28:31], v[68:71], a[252:255], v[28:31]\n"
    "v_mfma_f32_16x16x32_bf16 v[32:35], v[72:75], a[248:251], v[32:35]\n"
    "v_mfma_f32_16x16x32_bf16 v[36:39], v[72:75], a[252:255], v[36:39]\n"
    "v_mfma_f32_16x16x32_bf16 v[40:43], v[76:79], a[248:251], v[40:43]\n"
    "v_mfma_f32_16x16x32_bf16 v[44:47], v[76:79], a[252:255], v[44:47]\n"
    "s_nop 3\n"
    "s_mov_b32 s28, 0x8000\n"
    "s_mov_b32 s70, 0xffff0000\n"
    "v_cvt_pk_bf16_f32 v68, v24, v28\n"
    "v_cvt_pk_bf16_f32 v69, v25, v29\n"
    "v_cvt_pk_bf16_f32 v70, v26, v30\n"
    "v_cvt_pk_bf16_f32 v71, v27, v31\n"
    "v_min_u32 v68, -2, v68\n"
    "v_min_u32 v69, -2, v69\n"
    "v_min_u32 v70, -2, v70\n"
    "v_min_u32 v71, -2, v71\n"
    "buffer_store_dwordx4 v[68:71], v153, s[48:51], s7 offen offset:1024 sc1\n"
    "v_cvt_pk_bf16_f32 v72, v32, v36\n"
    "v_cvt_pk_bf16_f32 v73, v33, v37\n"
    "v_cvt_pk_bf16_f32 v74, v34, v38\n"
    "v_cvt_pk_bf16_f32 v75, v35, v39\n"
    "v_min_u32 v72, -2, v72\n"
    "v_min_u32 v73, -2, v73\n"
    "v_min_u32 v74, -2, v74\n"
    "v_min_u32 v75, -2, v75\n"
    "buffer_store_dwordx4 v[72:75], v153, s[48:51], s8 offen offset:2048 sc1\n"
    "v_cvt_pk_bf16_f32 v76, v40, v44\n"
    "v_cvt_pk_bf16_f32 v77, v41, v45\n"
    "v_cvt_pk_bf16_f32 v78, v42, v46\n"
    "v_cvt_pk_bf16_f32 v79, v43, v47\n"
    "v_min_u32 v76, -2, v76\n"
    "v_min_u32 v77, -2, v77\n"
    "v_min_u32 v78, -2, v78\n"
    "v_min_u32 v79, -2, v79\n"
    "buffer_store_dwordx4 v[76:79], v153, s[48:51], s9 offen offset:3072 sc1\n"
    "s_mov_b32 s79, 65536\n"
    ".Lp_poll:\n"
    "buffer_load_dwordx4 v[48:51], v153, s[48:51], s12 offen offset:1024 sc1\n"
    "buffer_load_dwordx4 v[52:55], v153, s[48:51], s12 offen offset:2048 sc1\n"
    "buffer_load_dwordx4 v[56:59], v153, s[48:51], s12 offen offset:3072 sc1\n"
    "s_waitcnt vmcnt(0)\n"
    "v_max3_u32 v1, v48, v49, v50\n"
    "v_max3_u32 v2, v51, v52, v53\n"
    "v_max3_u32 v1, v1, v54, v55\n"
    "v_max3_u32 v2, v2, v56, v57\n"
    "v_max3_u32 v1, v1, v58, v59\n"
    "v_max_u32 v1, v1, v2\n"
    "v_cmp_eq_u32 vcc, -1, v1\n"
    "s_sub_u32 s79, s79, 1\n"
    "s_cbranch_vccz .Lp_done\n"
    "s_cbranch_scc0 .Lp_poll\n"
    ".Lp_done:\n"
    "v_and_b32 v64, s70, v48\n"
    "v_and_b32 v65, s70, v49\n"
    "v_and_b32 v66, s70, v50\n"
    "v_and_b32 v67, s70, v51\n"
    "v_lshlrev_b32 v48, 16, v48\n"
    "v_lshlrev_b32 v49, 16, v49\n"
    "v_lshlrev_b32 v50, 16, v50\n"
    "v_lshlrev_b32 v51, 16, v51\n"
    "v_add_f32 v20, v20, v64\n"
    "v_add_f32 v21, v21, v65\n"
    "v_add_f32 v22, v22, v66\n"
    "v_add_f32 v23, v23, v67\n"
    "v_add_f32 v16, v16, v48\n"
    "v_add_f32 v17, v17, v49\n"
    "v_add_f32 v18, v18, v50\n"
    "v_add_f32 v19, v19, v51\n"
    "v_and_b32 v64, s70, v52\n"
    "v_and_b32 v65, s70, v53\n"
    "v_and_b32 v66, s70, v54\n"
    "v_and_b32 v67, s70, v55\n"
    "v_lshlrev_b32 v52, 16, v52\n"
    "v_lshlrev_b32 v53, 16, v53\n"
    "v_lshlrev_b32 v54, 16, v54\n"
    "v_lshlrev_b32 v55, 16, v55\n"
    "v_add_f32 v20, v20, v64\n"
    "v_add_f32 v21, v21, v65\n"
    "v_add_f32 v22, v22, v66\n"
    "v_add_f32 v23, v23, v67\n"
    "v_add_f32 v16, v16, v52\n"
    "v_add_f32 v17, v17, v53\n"
    "v_add_f32 v18, v18, v54\n"
    "v_add_f32 v19, v19, v55\n"
    "v_and_b32 v64, s70, v56\n"
    "v_and_b32 v65, s70, v57\n"
    "v_and_b32 v66, s70, v58\n"
    "v_and_b32 v67, s70, v59\n"
    "v_lshlrev_b32 v56, 16, v56\n"
    "v_lshlrev_b32 v57, 16, v57\n"
    "v_lshlrev_b32 v58, 16, v58\n"
    "v_lshlrev_b32 v59, 16, v59\n"
    "v_add_f32 v20, v20, v64\n"
    "v_add_f32 v21, v21, v65\n"
    "v_add_f32 v22, v22, v66\n"
    "v_add_f32 v23, v23, v67\n"
    "v_add_f32 v16, v16, v56\n"
    "v_add_f32 v17, v17, v57\n"
    "v_add_f32 v18, v18, v58\n"
    "v_add_f32 v19, v19, v59\n"
    "v_cvt_pk_bf16_f32 v4, v16, v17\n"
    "v_cvt_pk_bf16_f32 v5, v18, v19\n"
    "v_cvt_pk_bf16_f32 v6, v20, v21\n"
    "v_cvt_pk_bf16_f32 v7, v22, v23\n"
    "buffer_store_dwordx2 v[4:5], v158, s[60:63], 0 offen\n"
    "buffer_store_dwordx2 v[6:7], v158, s[60:63], s28 offen\n"
    "s_branch .Lend\n"
    ".Lrare_a1:\n"
    "s_call_b64 s[74:75], .Lsm0\n"
    "s_branch .Lback_a1\n"
    ".Lrare_b1:\n"
    "s_call_b64 s[74:75], .Lsm1\n"
    "s_branch .Lback_b1\n"
    ".Lrare_a2:\n"
    "s_call_b64 s[74:75], .Lsm0\n"
    "s_branch .Lback_a2\n"
    ".Lrare_b2:\n"
    "s_call_b64 s[74:75], .Lsm1\n"
    "s_branch .Lback_b2\n"
    ".Lrare_a3:\n"
    "s_call_b64 s[74:75], .Lsm0\n"
    "s_branch .Lback_a3\n"
    ".Lrare_b3:\n"
    "s_call_b64 s[74:75], .Lsm1\n"
    "s_branch .Lback_b3\n"
    ".Lrare_a0:\n"
    "s_call_b64 s[74:75], .Lsm0\n"
    "s_branch .Lback_a0\n"
    ".Lrare_b0:\n"
    "s_call_b64 s[74:75], .Lsm1\n"
    "s_branch .Lback_b0\n"
    ".Lrare_a3K:\n"
    "s_call_b64 s[74:75], .Lsm0\n"
    "s_branch .Lback_a3K\n"
    ".Lrare_b3K:\n"
    "s_call_b64 s[74:75], .Lsm1\n"
    "s_branch .Lback_b3K\n"
    ".Lrare_a0K:\n"
    "s_call_b64 s[74:75], .Lsm0\n"
    "s_branch .Lback_a0K\n"
    ".Lrare_b0K:\n"
    "s_call_b64 s[74:75], .Lsm1\n"
    "s_branch .Lback_b0K\n"
    ".Lrare_a1KV:\n"
    "s_call_b64 s[74:75], .Lsm0\n"
    "s_branch .Lback_a1KV\n"
    ".Lrare_b1KV:\n"
    "s_call_b64 s[74:75], .Lsm1\n"
    "s_branch .Lback_b1KV\n"
    ".Lrare_a2KV:\n"
    "s_call_b64 s[74:75], .Lsm0\n"
    "s_branch .Lback_a2KV\n"
    ".Lrare_b2KV:\n"
    "s_call_b64 s[74:75], .Lsm1\n"
    "s_branch .Lback_b2KV\n"
    ".Lrare_c:\n"
    "s_call_b64 s[74:75], .Lsm0\n"
    "s_branch .Lback_c\n"
    ".Lrare_d:\n"
    "s_call_b64 s[74:75], .Lsm1\n"
    "s_branch .Lback_d\n"
    ".Lwait_1:\n"
    "s_sleep 1\n"
    "s_memrealtime s[34:35]\n"
    "s_waitcnt lgkmcnt(0)\n"
    "s_sub_u32 s28, s34, s80\n"
    "s_cmp_lt_i32 s28, 0\n"
    "s_cbranch_scc1 .Lwait_1\n"
    "s_branch .Lgo_1\n"
    ".Lwait_2:\n"
    "s_sleep 1\n"
    "s_memrealtime s[34:35]\n"
    "s_waitcnt lgkmcnt(0)\n"
    "s_sub_u32 s28, s34, s80\n"
    "s_cmp_lt_i32 s28, 0\n"
    "s_cbranch_scc1 .Lwait_2\n"
    "s_branch .Lgo_2\n"
    ".Lwait_3:\n"
    "s_sleep 1\n"
    "s_memrealtime s[34:35]\n"
    "s_waitcnt lgkmcnt(0)\n"
    "s_sub_u32 s28, s34, s80\n"
    "s_cmp_lt_i32 s28, 0\n"
    "s_cbranch_scc1 .Lwait_3\n"
    "s_branch .Lgo_3\n"
    ".Lwait_0:\n"
    "s_sleep 1\n"
    "s_memrealtime s[34:35]\n"
    "s_waitcnt lgkmcnt(0)\n"
    "s_sub_u32 s28, s34, s80\n"
    "s_cmp_lt_i32 s28, 0\n"
    "s_cbranch_scc1 .Lwait_0\n"
    "s_branch .Lgo_0\n"
    ".Lwait_3K:\n"
    "s_sleep 1\n"
    "s_memrealtime s[34:35]\n"
    "s_waitcnt lgkmcnt(0)\n"
    "s_sub_u32 s28, s34, s80\n"
    "s_cmp_lt_i32 s28, 0\n"
    "s_cbranch_scc1 .Lwait_3K\n"
    "s_branch .Lgo_3K\n"
    ".Lwait_0K:\n"
    "s_sleep 1\n"
    "s_memrealtime s[34:35]\n"
    "s_waitcnt lgkmcnt(0)\n"
    "s_sub_u32 s28, s34, s80\n"
    "s_cmp_lt_i32 s28, 0\n"
    "s_cbranch_scc1 .Lwait_0K\n"
    "s_branch .Lgo_0K\n"
    ".Lwait_1KV:\n"
    "s_sleep 1\n"
    "s_memrealtime s[34:35]\n"
    "s_waitcnt lgkmcnt(0)\n"
    "s_sub_u32 s28, s34, s80\n"
    "s_cmp_lt_i32 s28, 0\n"
    "s_cbranch_scc1 .Lwait_1KV\n"
    "s_branch .Lgo_1KV\n"
    ".Lwait_2KV:\n"
    "s_sleep 1\n"
    "s_memrealtime s[34:35]\n"
    "s_waitcnt lgkmcnt(0)\n"
    "s_sub_u32 s28, s34, s80\n"
    "s_cmp_lt_i32 s28, 0\n"
    "s_cbranch_scc1 .Lwait_2KV\n"
    "s_branch .Lgo_2KV\n"
    ".Lsm0:\n"
    "s_nop 7\n"
    "v_max3_f32 v2, v16, v17, v18\n"
    "v_max3_f32 v3, v24, v25, v26\n"
    "v_max3_f32 v2, v2, v19, v20\n"
    "v_max3_f32 v3, v3, v27, v28\n"
    "v_max3_f32 v2, v2, v21, v22\n"
    "v_max3_f32 v3, v3, v29, v30\n"
    "v_max3_f32 v2, v2, v23, 0\n"
    "v_max3_f32 v3, v3, v31, 0\n"
    "v_mov_b32 v4, v2\n"
    "v_mov_b32 v5, v3\n"
    "s_nop 0\n"
    "v_permlane16_swap_b32 v4, v2\n"
    "v_permlane16_swap_b32 v5, v3\n"
    "v_max_f32 v2, v2, v4\n"
    "v_max_f32 v3, v3, v5\n"
    "v_mov_b32 v4, v2\n"
    "v_mov_b32 v5, v3\n"
    "s_nop 0\n"
    "v_permlane32_swap_b32 v4, v2\n"
    "v_permlane32_swap_b32 v5, v3\n"
    "v_max_f32 v2, v2, v4\n"
    "v_max_f32 v3, v3, v5\n"
    "v_add_f32 v138, v138, v2\n"
    "v_add_f32 v139, v139, v3\n"
    "v_exp_f32 v4, -v2\n"
    "v_exp_f32 v5, -v3\n"
    "s_nop 0\n"
    "v_mul_f32 v134, v134, v4\n"
    "v_mul_f32 v135, v135, v5\n"
    "v_pk_mul_f32 v[192:193], v[192:193], v[4:5] op_sel:[0,0] op_sel_hi:[1,0]\n"
    "v_pk_mul_f32 v[194:195], v[194:195], v[4:5] op_sel:[0,0] op_sel_hi:[1,0]\n"
    "v_pk_mul_f32 v[196:197], v[196:197], v[4:5] op_sel:[0,0] op_sel_hi:[1,0]\n"
    "v_pk_mul_f32 v[198:199], v[198:199], v[4:5] op_sel:[0,0] op_sel_hi:[1,0]\n"
    "v_pk_mul_f32 v[200:201], v[200:201], v[4:5] op_sel:[0,0] op_sel_hi:[1,0]\n"
    "v_pk_mul_f32 v[202:203], v[202:203], v[4:5] op_sel:[0,0] op_sel_hi:[1,0]\n"
    "v_pk_mul_f32 v[204:205], v[204:205], v[4:5] op_sel:[0,0] op_sel_hi:[1,0]\n"
    "v_pk_mul_f32 v[206:207], v[206:207], v[4:5] op_sel:[0,0] op_sel_hi:[1,0]\n"
    "v_pk_mul_f32 v[208:209], v[208:209], v[4:5] op_sel:[0,1] op_sel_hi:[1,1]\n"
    "v_pk_mul_f32 v[210:211], v[210:211], v[4:5] op_sel:[0,1] op_sel_hi:[1,1]\n"
    "v_pk_mul_f32 v[212:213], v[212:213], v[4:5] op_sel:[0,1] op_sel_hi:[1,1]\n"
    "v_pk_mul_f32 v[214:215], v[214:215], v[4:5] op_sel:[0,1] op_sel_hi:[1,1]\n"
    "v_pk_mul_f32 v[216:217], v[216:217], v[4:5] op_sel:[0,1] op_sel_hi:[1,1]\n"
    "v_pk_mul_f32 v[218:219], v[218:219], v[4:5] op_sel:[0,1] op_sel_hi:[1,1]\n"
    "v_pk_mul_f32 v[220:221], v[220:221], v[4:5] op_sel:[0,1] op_sel_hi:[1,1]\n"
    "v_pk_mul_f32 v[222:223], v[222:223], v[4:5] op_sel:[0,1] op_sel_hi:[1,1]\n"
    "v_accvgpr_read_b32 v8, a0\n"
    "v_accvgpr_read_b32 v9, a1\n"
    "v_accvgpr_read_b32 v10, a2\n"
    "v_accvgpr_read_b32 v11, a3\n"
    "v_pk_mul_f32 v[8:9], v[8:9], v[4:5] op_sel:[0,0] op_sel_hi:[1,0]\n"
    "v_pk_mul_f32 v[10:11], v[10:11], v[4:5] op_sel:[0,0] op_sel_hi:[1,0]\n"
    "v_accvgpr_write_b32 a0, v8\n"
    "v_accvgpr_write_b32 a1, v9\n"
    "v_accvgpr_write_b32 a2, v10\n"
    "v_accvgpr_write_b32 a3, v11\n"
    "v_accvgpr_read_b32 v8, a4\n"
    "v_accvgpr_read_b32 v9, a5\n"
    "v_accvgpr_read_b32 v10, a6\n"
    "v_accvgpr_read_b32 v11, a7\n"
    "v_pk_mul_f32 v[8:9], v[8:9], v[4:5] op_sel:[0,0] op_sel_hi:[1,0]\n"
    "v_pk_mul_f32 v[10:11], v[10:11], v[4:5] op_sel:[0,0] op_sel_hi:[1,0]\n"
    "v_accvgpr_write_b32 a4, v8\n"
    "v_accvgpr_write_b32 a5, v9\n"
    "v_accvgpr_write_b32 a6, v10\n"
    "v_accvgpr_write_b32 a7, v11\n"
    "v_accvgpr_read_b32 v8, a8\n"
    "v_accvgpr_read_b32 v9, a9\n"
    "v_accvgpr_read_b32 v10, a10\n"
    "v_accvgpr_read_b32 v11, a11\n"
    "v_pk_mul_f32 v[8:9], v[8:9], v[4:5] op_sel:[0,0] op_sel_hi:[1,0]\n"
    "v_pk_mul_f32 v[10:11], v[10:11], v[4:5] op_sel:[0,0] op_sel_hi:[1,0]\n"
    "v_accvgpr_write_b32 a8, v8\n"
    "v_accvgpr_write_b32 a9, v9\n"
    "v_accvgpr_write_b32 a10, v10\n"
    "v_accvgpr_write_b32 a11, v11\n"
    "v_accvgpr_read_b32 v8, a12\n"
    "v_accvgpr_read_b32 v9, a13\n"
    "v_accvgpr_read_b32 v10, a14\n"
    "v_accvgpr_read_b32 v11, a15\n"
    "v_pk_mul_f32 v[8:9], v[8:9], v[4:5] op_sel:[0,0] op_sel_hi:[1,0]\n"
    "v_pk_mul_f32 v[10:11], v[10:11], v[4:5] op_sel:[0,0] op_sel_hi:[1,0]\n"
    "v_accvgpr_write_b32 a12, v8\n"
    "v_accvgpr_write_b32 a13, v9\n"
    "v_accvgpr_write_b32 a14, v10\n"
    "v_accvgpr_write_b32 a15, v11\n"
    "v_accvgpr_read_b32 v8, a16\n"
    "v_accvgpr_read_b32 v9, a17\n"
    "v_accvgpr_read_b32 v10, a18\n"
    "v_accvgpr_read_b32 v11, a19\n"
    "v_pk_mul_f32 v[8:9], v[8:9], v[4:5] op_sel:[0,1] op_sel_hi:[1,1]\n"
    "v_pk_mul_f32 v[10:11], v[10:11], v[4:5] op_sel:[0,1] op_sel_hi:[1,1]\n"
    "v_accvgpr_write_b32 a16, v8\n"
    "v_accvgpr_write_b32 a17, v9\n"
    "v_accvgpr_write_b32 a18, v10\n"
    "v_accvgpr_write_b32 a19, v11\n"
    "v_accvgpr_read_b32 v8, a20\n"
    "v_accvgpr_read_b32 v9, a21\n"
    "v_accvgpr_read_b32 v10, a22\n"
    "v_accvgpr_read_b32 v11, a23\n"
    "v_pk_mul_f32 v[8:9], v[8:9], v[4:5] op_sel:[0,1] op_sel_hi:[1,1]\n"
    "v_pk_mul_f32 v[10:11], v[10:11], v[4:5] op_sel:[0,1] op_sel_hi:[1,1]\n"
    "v_accvgpr_write_b32 a20, v8\n"
    "v_accvgpr_write_b32 a21, v9\n"
    "v_accvgpr_write_b32 a22, v10\n"
    "v_accvgpr_write_b32 a23, v11\n"
    "v_accvgpr_read_b32 v8, a24\n"
    "v_accvgpr_read_b32 v9, a25\n"
    "v_accvgpr_read_b32 v10, a26\n"
    "v_accvgpr_read_b32 v11, a27\n"
    "v_pk_mul_f32 v[8:9], v[8:9], v[4:5] op_sel:[0,1] op_sel_hi:[1,1]\n"
    "v_pk_mul_f32 v[10:11], v[10:11], v[4:5] op_sel:[0,1] op_sel_hi:[1,1]\n"
    "v_accvgpr_write_b32 a24, v8\n"
    "v_accvgpr_write_b32 a25, v9\n"
    "v_accvgpr_write_b32 a26, v10\n"
    "v_accvgpr_write_b32 a27, v11\n"
    "v_accvgpr_read_b32 v8, a28\n"
    "v_accvgpr_read_b32 v9, a29\n"
    "v_accvgpr_read_b32 v10, a30\n"
    "v_accvgpr_read_b32 v11, a31\n"
    "v_pk_mul_f32 v[8:9], v[8:9], v[4:5] op_sel:[0,1] op_sel_hi:[1,1]\n"
    "v_pk_mul_f32 v[10:11], v[10:11], v[4:5] op_sel:[0,1] op_sel_hi:[1,1]\n"
    "v_accvgpr_write_b32 a28, v8\n"
    "v_accvgpr_write_b32 a29, v9\n"
    "v_accvgpr_write_b32 a30, v10\n"
    "v_accvgpr_write_b32 a31, v11\n"
    "v_sub_f32 v16, v16, v2\n"
    "v_sub_f32 v17, v17, v2\n"
    "v_sub_f32 v18, v18, v2\n"
    "v_sub_f32 v19, v19, v2\n"
    "v_sub_f32 v20, v20, v2\n"
    "v_sub_f32 v21, v21, v2\n"
    "v_sub_f32 v22, v22, v2\n"
    "v_sub_f32 v23, v23, v2\n"
    "v_sub_f32 v24, v24, v3\n"
    "v_sub_f32 v25, v25, v3\n"
    "v_sub_f32 v26, v26, v3\n"
    "v_sub_f32 v27, v27, v3\n"
    "v_sub_f32 v28, v28, v3\n"
    "v_sub_f32 v29, v29, v3\n"
    "v_sub_f32 v30, v30, v3\n"
    "v_sub_f32 v31, v31, v3\n"
    "v_sub_f32 v80, 0, v138\n"
    "v_sub_f32 v81, 0, v138\n"
    "v_sub_f32 v82, 0, v138\n"
    "v_sub_f32 v83, 0, v138\n"
    "v_sub_f32 v84, 0, v139\n"
    "v_sub_f32 v85, 0, v139\n"
    "v_sub_f32 v86, 0, v139\n"
    "v_sub_f32 v87, 0, v139\n"
    "v_exp_f32 v48, v16\n"
    "v_exp_f32 v49, v17\n"
    "v_exp_f32 v50, v18\n"
    "v_exp_f32 v51, v19\n"
    "v_exp_f32 v52, v20\n"
    "v_exp_f32 v53, v21\n"
    "v_exp_f32 v54, v22\n"
    "v_exp_f32 v55, v23\n"
    "v_exp_f32 v56, v24\n"
    "v_exp_f32 v57, v25\n"
    "v_exp_f32 v58, v26\n"
    "v_exp_f32 v59, v27\n"
    "v_exp_f32 v60, v28\n"
    "v_exp_f32 v61, v29\n"
    "v_exp_f32 v62, v30\n"
    "v_exp_f32 v63, v31\n"
    "v_add_f32 v130, v48, v49\n"
    "v_add_f32 v131, v56, v57\n"
    "v_add_f32 v130, v130, v50\n"
    "v_add_f32 v131, v131, v58\n"
    "v_add_f32 v130, v130, v51\n"
    "v_add_f32 v131, v131, v59\n"
    "v_add_f32 v130, v130, v52\n"
    "v_add_f32 v131, v131, v60\n"
    "v_add_f32 v130, v130, v53\n"
    "v_add_f32 v131, v131, v61\n"
    "v_add_f32 v130, v130, v54\n"
    "v_add_f32 v131, v131, v62\n"
    "v_add_f32 v130, v130, v55\n"
    "v_add_f32 v131, v131, v63\n"
    "v_cvt_pk_bf16_f32 v96, v48, v49\n"
    "v_cvt_pk_bf16_f32 v97, v50, v51\n"
    "v_cvt_pk_bf16_f32 v98, v52, v53\n"
    "v_cvt_pk_bf16_f32 v99, v54, v55\n"
    "v_cvt_pk_bf16_f32 v100, v56, v57\n"
    "v_cvt_pk_bf16_f32 v101, v58, v59\n"
    "v_cvt_pk_bf16_f32 v102, v60, v61\n"
    "v_cvt_pk_bf16_f32 v103, v62, v63\n"
    "s_nop 1\n"
    "s_setpc_b64 s[74:75]\n"
    ".Lsm1:\n"
    "s_nop 7\n"
    "v_max3_f32 v2, v32, v33, v34\n"
    "v_max3_f32 v3, v40, v41, v42\n"
    "v_max3_f32 v2, v2, v35, v36\n"
    "v_max3_f32 v3, v3, v43, v44\n"
    "v_max3_f32 v2, v2, v37, v38\n"
    "v_max3_f32 v3, v3, v45, v46\n"
    "v_max3_f32 v2, v2, v39, 0\n"
    "v_max3_f32 v3, v3, v47, 0\n"
    "v_mov_b32 v4, v2\n"
    "v_mov_b32 v5, v3\n"
    "s_nop 0\n"
    "v_permlane16_swap_b32 v4, v2\n"
    "v_permlane16_swap_b32 v5, v3\n"
    "v_max_f32 v2, v2, v4\n"
    "v_max_f32 v3, v3, v5\n"
    "v_mov_b32 v4, v2\n"
    "v_mov_b32 v5, v3\n"
    "s_nop 0\n"
    "v_permlane32_swap_b32 v4, v2\n"
    "v_permlane32_swap_b32 v5, v3\n"
    "v_max_f32 v2, v2, v4\n"
    "v_max_f32 v3, v3, v5\n"
    "v_add_f32 v140, v140, v2\n"
    "v_add_f32 v141, v141, v3\n"
    "v_exp_f32 v4, -v2\n"
    "v_exp_f32 v5, -v3\n"
    "s_nop 0\n"
    "v_mul_f32 v136, v136, v4\n"
    "v_mul_f32 v137, v137, v5\n"
    "v_pk_mul_f32 v[224:225], v[224:225], v[4:5] op_sel:[0,0] op_sel_hi:[1,0]\n"
    "v_pk_mul_f32 v[226:227], v[226:227], v[4:5] op_sel:[0,0] op_sel_hi:[1,0]\n"
    "v_pk_mul_f32 v[228:229], v[228:229], v[4:5] op_sel:[0,0] op_sel_hi:[1,0]\n"
    "v_pk_mul_f32 v[230:231], v[230:231], v[4:5] op_sel:[0,0] op_sel_hi:[1,0]\n"
    "v_pk_mul_f32 v[232:233], v[232:233], v[4:5] op_sel:[0,0] op_sel_hi:[1,0]\n"
    "v_pk_mul_f32 v[234:235], v[234:235], v[4:5] op_sel:[0,0] op_sel_hi:[1,0]\n"
    "v_pk_mul_f32 v[236:237], v[236:237], v[4:5] op_sel:[0,0] op_sel_hi:[1,0]\n"
    "v_pk_mul_f32 v[238:239], v[238:239], v[4:5] op_sel:[0,0] op_sel_hi:[1,0]\n"
    "v_pk_mul_f32 v[240:241], v[240:241], v[4:5] op_sel:[0,1] op_sel_hi:[1,1]\n"
    "v_pk_mul_f32 v[242:243], v[242:243], v[4:5] op_sel:[0,1] op_sel_hi:[1,1]\n"
    "v_pk_mul_f32 v[244:245], v[244:245], v[4:5] op_sel:[0,1] op_sel_hi:[1,1]\n"
    "v_pk_mul_f32 v[246:247], v[246:247], v[4:5] op_sel:[0,1] op_sel_hi:[1,1]\n"
    "v_pk_mul_f32 v[248:249], v[248:249], v[4:5] op_sel:[0,1] op_sel_hi:[1,1]\n"
    "v_pk_mul_f32 v[250:251], v[250:251], v[4:5] op_sel:[0,1] op_sel_hi:[1,1]\n"
    "v_pk_mul_f32 v[252:253], v[252:253], v[4:5] op_sel:[0,1] op_sel_hi:[1,1]\n"
    "v_pk_mul_f32 v[254:255], v[254:255], v[4:5] op_sel:[0,1] op_sel_hi:[1,1]\n"
    "v_accvgpr_read_b32 v8, a32\n"
    "v_accvgpr_read_b32 v9, a33\n"
    "v_accvgpr_read_b32 v10, a34\n"
    "v_accvgpr_read_b32 v11, a35\n"
    "v_pk_mul_f32 v[8:9], v[8:9], v[4:5] op_sel:[0,0] op_sel_hi:[1,0]\n"
    "v_pk_mul_f32 v[10:11], v[10:11], v[4:5] op_sel:[0,0] op_sel_hi:[1,0]\n"
    "v_accvgpr_write_b32 a32, v8\n"
    "v_accvgpr_write_b32 a33, v9\n"
    "v_accvgpr_write_b32 a34, v10\n"
    "v_accvgpr_write_b32 a35, v11\n"
    "v_accvgpr_read_b32 v8, a36\n"
    "v_accvgpr_read_b32 v9, a37\n"
    "v_accvgpr_read_b32 v10, a38\n"
    "v_accvgpr_read_b32 v11, a39\n"
    "v_pk_mul_f32 v[8:9], v[8:9], v[4:5] op_sel:[0,0] op_sel_hi:[1,0]\n"
    "v_pk_mul_f32 v[10:11], v[10:11], v[4:5] op_sel:[0,0] op_sel_hi:[1,0]\n"
    "v_accvgpr_write_b32 a36, v8\n"
    "v_accvgpr_write_b32 a37, v9\n"
    "v_accvgpr_write_b32 a38, v10\n"
    "v_accvgpr_write_b32 a39, v11\n"
    "v_accvgpr_read_b32 v8, a40\n"
    "v_accvgpr_read_b32 v9, a41\n"
    "v_accvgpr_read_b32 v10, a42\n"
    "v_accvgpr_read_b32 v11, a43\n"
    "v_pk_mul_f32 v[8:9], v[8:9], v[4:5] op_sel:[0,0] op_sel_hi:[1,0]\n"
    "v_pk_mul_f32 v[10:11], v[10:11], v[4:5] op_sel:[0,0] op_sel_hi:[1,0]\n"
    "v_accvgpr_write_b32 a40, v8\n"
    "v_accvgpr_write_b32 a41, v9\n"
    "v_accvgpr_write_b32 a42, v10\n"
    "v_accvgpr_write_b32 a43, v11\n"
    "v_accvgpr_read_b32 v8, a44\n"
    "v_accvgpr_read_b32 v9, a45\n"
    "v_accvgpr_read_b32 v10, a46\n"
    "v_accvgpr_read_b32 v11, a47\n"
    "v_pk_mul_f32 v[8:9], v[8:9], v[4:5] op_sel:[0,0] op_sel_hi:[1,0]\n"
    "v_pk_mul_f32 v[10:11], v[10:11], v[4:5] op_sel:[0,0] op_sel_hi:[1,0]\n"
    "v_accvgpr_write_b32 a44, v8\n"
    "v_accvgpr_write_b32 a45, v9\n"
    "v_accvgpr_write_b32 a46, v10\n"
    "v_accvgpr_write_b32 a47, v11\n"
    "v_accvgpr_read_b32 v8, a48\n"
    "v_accvgpr_read_b32 v9, a49\n"
    "v_accvgpr_read_b32 v10, a50\n"
    "v_accvgpr_read_b32 v11, a51\n"
    "v_pk_mul_f32 v[8:9], v[8:9], v[4:5] op_sel:[0,1] op_sel_hi:[1,1]\n"
    "v_pk_mul_f32 v[10:11], v[10:11], v[4:5] op_sel:[0,1] op_sel_hi:[1,1]\n"
    "v_accvgpr_write_b32 a48, v8\n"
    "v_accvgpr_write_b32 a49, v9\n"
    "v_accvgpr_write_b32 a50, v10\n"
    "v_accvgpr_write_b32 a51, v11\n"
    "v_accvgpr_read_b32 v8, a52\n"
    "v_accvgpr_read_b32 v9, a53\n"
    "v_accvgpr_read_b32 v10, a54\n"
    "v_accvgpr_read_b32 v11, a55\n"
    "v_pk_mul_f32 v[8:9], v[8:9], v[4:5] op_sel:[0,1] op_sel_hi:[1,1]\n"
    "v_pk_mul_f32 v[10:11], v[10:11], v[4:5] op_sel:[0,1] op_sel_hi:[1,1]\n"
    "v_accvgpr_write_b32 a52, v8\n"
    "v_accvgpr_write_b32 a53, v9\n"
    "v_accvgpr_write_b32 a54, v10\n"
    "v_accvgpr_write_b32 a55, v11\n"
    "v_accvgpr_read_b32 v8, a56\n"
    "v_accvgpr_read_b32 v9, a57\n"
    "v_accvgpr_read_b32 v10, a58\n"
    "v_accvgpr_read_b32 v11, a59\n"
    "v_pk_mul_f32 v[8:9], v[8:9], v[4:5] op_sel:[0,1] op_sel_hi:[1,1]\n"
    "v_pk_mul_f32 v[10:11], v[10:11], v[4:5] op_sel:[0,1] op_sel_hi:[1,1]\n"
    "v_accvgpr_write_b32 a56, v8\n"
    "v_accvgpr_write_b32 a57, v9\n"
    "v_accvgpr_write_b32 a58, v10\n"
    "v_accvgpr_write_b32 a59, v11\n"
    "v_accvgpr_read_b32 v8, a60\n"
    "v_accvgpr_read_b32 v9, a61\n"
    "v_accvgpr_read_b32 v10, a62\n"
    "v_accvgpr_read_b32 v11, a63\n"
    "v_pk_mul_f32 v[8:9], v[8:9], v[4:5] op_sel:[0,1] op_sel_hi:[1,1]\n"
    "v_pk_mul_f32 v[10:11], v[10:11], v[4:5] op_sel:[0,1] op_sel_hi:[1,1]\n"
    "v_accvgpr_write_b32 a60, v8\n"
    "v_accvgpr_write_b32 a61, v9\n"
    "v_accvgpr_write_b32 a62, v10\n"
    "v_accvgpr_write_b32 a63, v11\n"
    "v_sub_f32 v32, v32, v2\n"
    "v_sub_f32 v33, v33, v2\n"
    "v_sub_f32 v34, v34, v2\n"
    "v_sub_f32 v35, v35, v2\n"
    "v_sub_f32 v36, v36, v2\n"
    "v_sub_f32 v37, v37, v2\n"
    "v_sub_f32 v38, v38, v2\n"
    "v_sub_f32 v39, v39, v2\n"
    "v_sub_f32 v40, v40, v3\n"
    "v_sub_f32 v41, v41, v3\n"
    "v_sub_f32 v42, v42, v3\n"
    "v_sub_f32 v43, v43, v3\n"
    "v_sub_f32 v44, v44, v3\n"
    "v_sub_f32 v45, v45, v3\n"
    "v_sub_f32 v46, v46, v3\n"
    "v_sub_f32 v47, v47, v3\n"
    "v_sub_f32 v88, 0, v140\n"
    "v_sub_f32 v89, 0, v140\n"
    "v_sub_f32 v90, 0, v140\n"
    "v_sub_f32 v91, 0, v140\n"
    "v_sub_f32 v92, 0, v141\n"
    "v_sub_f32 v93, 0, v141\n"
    "v_sub_f32 v94, 0, v141\n"
    "v_sub_f32 v95, 0, v141\n"
    "v_exp_f32 v64, v32\n"
    "v_exp_f32 v65, v33\n"
    "v_exp_f32 v66, v34\n"
    "v_exp_f32 v67, v35\n"
    "v_exp_f32 v68, v36\n"
    "v_exp_f32 v69, v37\n"
    "v_exp_f32 v70, v38\n"
    "v_exp_f32 v71, v39\n"
    "v_exp_f32 v72, v40\n"
    "v_exp_f32 v73, v41\n"
    "v_exp_f32 v74, v42\n"
    "v_exp_f32 v75, v43\n"
    "v_exp_f32 v76, v44\n"
    "v_exp_f32 v77, v45\n"
    "v_exp_f32 v78, v46\n"
    "v_exp_f32 v79, v47\n"
    "v_add_f32 v132, v64, v65\n"
    "v_add_f32 v133, v72, v73\n"
    "v_add_f32 v132, v132, v66\n"
    "v_add_f32 v133, v133, v74\n"
    "v_add_f32 v132, v132, v67\n"
    "v_add_f32 v133, v133, v75\n"
    "v_add_f32 v132, v132, v68\n"
    "v_add_f32 v133, v133, v76\n"
    "v_add_f32 v132, v132, v69\n"
    "v_add_f32 v133, v133, v77\n"
    "v_add_f32 v132, v132, v70\n"
    "v_add_f32 v133, v133, v78\n"
    "v_add_f32 v132, v132, v71\n"
    "v_add_f32 v133, v133, v79\n"
    "v_cvt_pk_bf16_f32 v104, v64, v65\n"
    "v_cvt_pk_bf16_f32 v105, v66, v67\n"
    "v_cvt_pk_bf16_f32 v106, v68, v69\n"
    "v_cvt_pk_bf16_f32 v107, v70, v71\n"
    "v_cvt_pk_bf16_f32 v108, v72, v73\n"
    "v_cvt_pk_bf16_f32 v109, v74, v75\n"
    "v_cvt_pk_bf16_f32 v110, v76, v77\n"
    "v_cvt_pk_bf16_f32 v111, v78, v79\n"
    "s_nop 1\n"
    "s_setpc_b64 s[74:75]\n"
    ".Lend:\n"
    :
    : [ka] "s"((unsigned long long)__builtin_amdgcn_kernarg_segment_ptr()), [bid] "s"(blockIdx.x), [tid] "v"(threadIdx.x), [lds] "s"((unsigned)(size_t)lds)
    :
      "v1", "v2", "v3", "v4", "v5", "v6", "v7", "v8", "v9", "v10", "v11", "v12", "v13", "v14", "v15", "v16", "v17", "v18", "v19", "v20", "v21", "v22", "v23", "v24", "v25", "v26", "v27", "v28", "v29", "v30", "v31", "v32",
      "v33", "v34", "v35", "v36", "v37", "v38", "v39", "v40", "v41", "v42", "v43", "v44", "v45", "v46", "v47", "v48", "v49", "v50", "v51", "v52", "v53", "v54", "v55", "v56", "v57", "v58", "v59", "v60", "v61", "v62", "v63", "v64",
      "v65", "v66", "v67", "v68", "v69", "v70", "v71", "v72", "v73", "v74", "v75", "v76", "v77", "v78", "v79", "v80", "v81", "v82", "v83", "v84", "v85", "v86", "v87", "v88", "v89", "v90", "v91", "v92", "v93", "v94", "v95", "v96",
      "v97", "v98", "v99", "v100", "v101", "v102", "v103", "v104", "v105", "v106", "v107", "v108", "v109", "v110", "v111", "v112", "v113", "v114", "v115", "v116", "v117", "v118", "v119", "v120", "v121", "v122", "v123", "v124", "v125", "v126", "v127", "v128",
      "v129", "v130", "v131", "v132", "v133", "v134", "v135", "v136", "v137", "v138", "v139", "v140", "v141", "v142", "v143", "v144", "v145", "v146", "v147", "v148", "v149", "v150", "v151", "v152", "v153", "v154", "v155", "v156", "v157", "v158", "v159", "v160",
      "v161", "v162", "v163", "v164", "v165", "v166", "v167", "v168", "v169", "v170", "v171", "v172", "v173", "v174", "v175", "v176", "v177", "v178", "v179", "v180", "v181", "v182", "v183", "v184", "v185", "v186", "v187", "v188", "v189", "v190", "v191", "v192",
      "v193", "v194", "v195", "v196", "v197", "v198", "v199", "v200", "v201", "v202", "v203", "v204", "v205", "v206", "v207", "v208", "v209", "v210", "v211", "v212", "v213", "v214", "v215", "v216", "v217", "v218", "v219", "v220", "v221", "v222", "v223", "v224",
      "v225", "v226", "v227", "v228", "v229", "v230", "v231", "v232", "v233", "v234", "v235", "v236", "v237", "v238", "v239", "v240", "v241", "v242", "v243", "v244", "v245", "v246", "v247", "v248", "v249", "v250", "v251", "v252", "v253", "v254", "v255", "a0",
      "a1", "a2", "a3", "a4", "a5", "a6", "a7", "a8", "a9", "a10", "a11", "a12", "a13", "a14", "a15", "a16", "a17", "a18", "a19", "a20", "a21", "a22", "a23", "a24", "a25", "a26", "a27", "a28", "a29", "a30", "a31", "a32",
      "a33", "a34", "a35", "a36", "a37", "a38", "a39", "a40", "a41", "a42", "a43", "a44", "a45", "a46", "a47", "a48", "a49", "a50", "a51", "a52", "a53", "a54", "a55", "a56", "a57", "a58", "a59", "a60", "a61", "a62", "a63", "a64",
      "a65", "a66", "a67", "a68", "a69", "a70", "a71", "a72", "a73", "a74", "a75", "a76", "a77", "a78", "a79", "a80", "a81", "a82", "a83", "a84", "a85", "a86", "a87", "a88", "a89", "a90", "a91", "a92", "a93", "a94", "a95", "a96",
      "a97", "a98", "a99", "a100", "a101", "a102", "a103", "a104", "a105", "a106", "a107", "a108", "a109", "a110", "a111", "a112", "a113", "a114", "a115", "a116", "a117", "a118", "a119", "a120", "a121", "a122", "a123", "a124", "a125", "a126", "a127", "a128",
      "a129", "a130", "a131", "a132", "a133", "a134", "a135", "a136", "a137", "a138", "a139", "a140", "a141", "a142", "a143", "a144", "a145", "a146", "a147", "a148", "a149", "a150", "a151", "a152", "a153", "a154", "a155", "a156", "a157", "a158", "a159", "a160",
      "a161", "a162", "a163", "a164", "a165", "a166", "a167", "a168", "a169", "a170", "a171", "a172", "a173", "a174", "a175", "a176", "a177", "a178", "a179", "a180", "a181", "a182", "a183", "a184", "a185", "a186", "a187", "a188", "a189", "a190", "a191", "a192",
      "a193", "a194", "a195", "a196", "a197", "a198", "a199", "a200", "a201", "a202", "a203", "a204", "a205", "a206", "a207", "a208", "a209", "a210", "a211", "a212", "a213", "a214", "a215", "a216", "a217", "a218", "a219", "a220", "a221", "a222", "a223", "a224",
      "a225", "a226", "a227", "a228", "a229", "a230", "a231", "a232", "a233", "a234", "a235", "a236", "a237", "a238", "a239", "a240", "a241", "a242", "a243", "a244", "a245", "a246", "a247", "a248", "a249", "a250", "a251", "a252", "a253", "a254", "a255", "s4",
      "s5", "s6", "s7", "s8", "s9", "s10", "s11", "s12", "s13", "s14", "s15", "s16", "s17", "s18", "s19", "s20", "s21", "s22", "s23", "s24", "s25", "s26", "s27", "s28", "s29", "s30", "s31", "s34", "s35", "s36", "s37", "s38",
      "s39", "s40", "s41", "s42", "s43", "s44", "s45", "s46", "s47", "s48", "s49", "s50", "s51", "s52", "s53", "s54", "s55", "s56", "s57", "s58", "s59", "s60", "s61", "s62", "s63", "s64", "s65", "s66", "s67", "s68", "s69", "s70",
      "s71", "s72", "s73", "s74", "s75", "s76", "s77", "s78", "s79", "s80", "s81", "s82", "s83", "s84", "s85", "s86", "s87", "s88", "s89", "s90", "s91", "s92", "s93", "s94", "s95", "s96", "s97", "s98", "s99", "vcc", "scc", "memory");
}

extern "C" void kernel_launch(void* const* d_in, const int* in_sizes, int n_in, void* d_out, int out_size, void* d_ws, size_t ws_size, hipStream_t stream) {
  gqa_block_fused<<<256, 256, 0, stream>>>(d_in[0], d_in[1], d_in[2], d_in[3], d_in[4], d_out, d_ws);
}
```
